```python
import torch

def _softmax(x, axis=-1):
    return torch.softmax(x, dim=axis)
def _gelu(x):
    return torch.nn.functional.gelu(x, approximate="tanh")
def _silu(x):
    return torch.nn.functional.silu(x)
def _sigmoid(x):
    return torch.sigmoid(x)
def _layernorm(x, axis=-1, eps=1e-5):
    m = x.mean(dim=axis, keepdim=True)
    v = x.var(dim=axis, keepdim=True, unbiased=False)
    return (x - m) / torch.sqrt(v + eps)
def _scatter_add(tgt, idx, src, axis=0):
    return tgt.clone().index_add_(axis, idx, src)
def _segment_sum(src, seg, n):
    out = torch.zeros((n,) + src.shape[1:], dtype=src.dtype)
    return out.index_add_(0, seg, src)
def _pad(x, pw):
    flat = tuple(v for pair in reversed(pw) for v in pair)
    return torch.nn.functional.pad(x, flat)

kernel_name = 'concat_heads__mha_block__softmax_full_mha__prompt'

def setup_inputs():

    _w = (lambda a: (lambda t: t.to(torch.bfloat16) if t.is_floating_point() else t)(torch.as_tensor(a)))
    def _A_setup():
        B, S, D, H = 1, 2048, 640, 32
        return {
            "x": _w((lambda *s: torch.randn(*s, dtype=torch.bfloat16) * 0.2)(B, S, D)),
            "wqkv": _w((lambda *s: torch.randn(*s, dtype=torch.bfloat16) * 0.2)(D, 3 * D)),
            "wo": _w((lambda *s: torch.randn(*s, dtype=torch.bfloat16) * 0.05)(D, D)),
        }
    _A_in = _A_setup()

    def _B_setup():
        B, S, D = 1, 2048, 640
        return {
            "x": _w((lambda *s: torch.randn(*s, dtype=torch.bfloat16) * 0.2)(B, S, D)),
            "wqkv": _w((lambda *s: torch.randn(*s, dtype=torch.bfloat16) * 0.2)(D, 3 * D)),
            "wo": _w((lambda *s: torch.randn(*s, dtype=torch.bfloat16) * 0.05)(D, D)),
        }
    _B_in = _B_setup()

    return {
        "A_x": _A_in["x"],
        "A_wqkv": _A_in["wqkv"],
        "A_wo": _A_in["wo"],
        "B_x": _B_in["x"],
        "B_wqkv": _B_in["wqkv"],
        "B_wo": _B_in["wo"],
    }

def reference(A_x, A_wqkv, A_wo, B_x, B_wqkv, B_wo):
    def _A_ref(x, wqkv, wo):
        B, S, D = x.shape
        H = 32
        Dh = D // H
        qkv = (lambda x, *d: x.permute(*d))((x @ wqkv).reshape(B, S, 3, H, Dh), 2, 0, 3, 1, 4)
        q, k, v = qkv[0], qkv[1], qkv[2]
        scores = torch.einsum("bhqd,bhkd->bhqk", q, k) * (Dh ** -0.5)
        p = _softmax(scores, axis=-1)
        o = torch.einsum("bhqk,bhkd->bhqd", p, v)
        return (lambda x, *d: x.permute(*d))(o, 0, 2, 1, 3).reshape(B, S, D) @ wo

    def _B_ref(x, wqkv, wo):
        B, S, D = x.shape
        H = 32; Dh = D // H
        qkv = (lambda x, *d: x.permute(*d))((x @ wqkv).reshape(B, S, 3, H, Dh), 2, 0, 3, 1, 4)
        q, k, v = qkv[0], qkv[1], qkv[2]
        scale = Dh ** -0.5
        scores = torch.einsum("bhqd,bhkd->bhqk", q, k) * scale
        p = _softmax(scores, axis=-1)
        o = torch.einsum("bhqk,bhkd->bhqd", p, v)
        return (lambda x, *d: x.permute(*d))(o, 0, 2, 1, 3).reshape(B, S, D) @ wo

    a = _A_ref(A_x, A_wqkv, A_wo)
    b = _B_ref(B_x, B_wqkv, B_wo)
    return torch.concatenate([a, b], axis=-1)
```

```cpp
#include <hip/hip_runtime.h>
#include <hip/hip_bf16.h>
#include <stdint.h>

typedef float f32x16 __attribute__((ext_vector_type(16)));
typedef __bf16 bf16x8 __attribute__((ext_vector_type(8)));
typedef __bf16 bf16x16 __attribute__((ext_vector_type(16)));
typedef short s16x4 __attribute__((ext_vector_type(4)));
typedef float f32x8 __attribute__((ext_vector_type(8)));
typedef unsigned u32x4 __attribute__((ext_vector_type(4)));
typedef unsigned u32x2 __attribute__((ext_vector_type(2)));
typedef unsigned u32x16 __attribute__((ext_vector_type(16)));
typedef unsigned long long u64;
#define LDSP __attribute__((address_space(3)))
#define AUX_SC1 16

#define LOADER_G(tid, kp, bid, ep) \
  asm volatile( \
    "v_readfirstlane_b32 s75, v0\n\t" \
    "s_load_dwordx8 s[76:83], s[0:1], 0x0\n\t" \
    "s_load_dwordx2 s[64:65], s[0:1], 0x30\n\t" \
    "s_lshr_b32 s75, s75, 6\n\t" \
    "s_cmp_ge_u32 s75, 4\n\t" \
    "s_cbranch_scc1 LGE_%=\n\t" \
    "v_and_b32 v30, 63, v0\n\t" \
    "s_bfe_u32 s66, s2, 0x30003\n\t" \
    "s_lshl_b32 s67, s66, 2\n\t" \
    "s_lshr_b32 s67, 0x37265140, s67\n\t" \
    "s_and_b32 s67, s67, 7\n\t" \
    "s_and_b32 s68, s2, 1\n\t" \
    "s_lshl3_add_u32 s66, s68, s66\n\t" \
    "s_lshr_b32 s72, s2, 6\n\t" \
    "s_and_b32 s61, s2, 2\n\t" \
    "s_lshl1_add_u32 s61, s61, s72\n\t" \
    "v_lshrrev_b32 v31, 3, v30\n\t" \
    "s_lshl_b32 s71, s75, 6\n\t" \
    "v_or_b32 v31, s71, v31\n\t" \
    "v_mul_u32_u24 v31, 0x500, v31\n\t" \
    "v_lshrrev_b32 v32, 4, v30\n\t" \
    "v_xor_b32 v33, v32, v30\n\t" \
    "v_and_b32 v33, 7, v33\n\t" \
    "v_lshl_or_b32 v2, v33, 4, v31\n\t" \
    "v_xor_b32 v3, 64, v2\n\t" \
    "v_add_u32 v3, 9216, v3\n\t" \
    "v_and_b32 v31, 1, v32\n\t" \
    "v_and_b32 v33, 3, v30\n\t" \
    "v_lshl_or_b32 v31, v31, 2, v33\n\t" \
    "v_or_b32 v31, 0x0c0c0c00, v31\n\t" \
    "v_mov_b32 v33, 0x01030002\n\t" \
    "s_mov_b32 s71, 0x53515250\n\t" \
    "v_perm_b32 v33, s71, v33, v31\n\t" \
    "v_mov_b32 v8, 0xa1a0a0a2\n\t" \
    "s_mov_b32 s71, 0xa4a35404\n\t" \
    "v_perm_b32 v31, s71, v8, v31\n\t" \
    "v_cmp_gt_u32 vcc, 32, v30\n\t" \
    "s_mul_i32 s70, s66, 80\n\t" \
    "s_mul_i32 s71, s75, 61440\n\t" \
    "s_add_u32 s70, s70, s71\n\t" \
    "v_cndmask_b32 v31, v31, v33, vcc\n\t" \
    "v_bfe_u32 v33, v30, 2, 2\n\t" \
    "v_mul_u32_u24 v33, 0xf00, v33\n\t" \
    "v_lshl_add_u32 v31, v31, 4, v33\n\t" \
    "v_add_u32 v8, s70, v31\n\t" \
    "s_bitcmp1_b32 s75, 0\n\t" \
    "s_cbranch_scc1 LPW_%=\n\t" \
    "s_lshl_b32 s71, s67, 6\n\t" \
    "v_or_b32 v31, s71, v30\n\t" \
    "v_mul_u32_u24 v9, 0x500, v31\n\t" \
    "s_bfe_u32 s71, s2, 0x30003\n\t" \
    "s_lshl_b32 s71, s71, 5\n\t" \
    "v_add_u32 v31, s71, v30\n\t" \
    "v_mul_u32_u24 v31, 0x500, v31\n\t" \
    "v_mov_b32 v33, 0x7f000000\n\t" \
    "v_cndmask_b32 v11, v33, v31, vcc\n\t" \
    "s_cmp_lt_u32 s67, 4\n\t" \
    "s_cselect_b32 s51, 0, 0x7f000000\n\t" \
    "s_movk_i32 s49, 128\n\t" \
    "s_branch LPE_%=\n\t" \
    "LPW_%=:\n\t" \
    "v_and_b32 v31, 15, v30\n\t" \
    "v_mul_u32_u24 v33, 52, v31\n\t" \
    "v_lshrrev_b32 v33, 8, v33\n\t" \
    "v_mul_u32_u24 v33, 0x280, v33\n\t" \
    "v_lshl_add_u32 v33, v31, 7, v33\n\t" \
    "v_mul_u32_u24 v9, 0xf00, v32\n\t" \
    "v_add_u32 v9, v9, v33\n\t" \
    "v_cmp_ne_u32 s[48:49], 15, v31\n\t" \
    "s_lshr_b32 s71, s2, 3\n\t" \
    "s_mul_i32 s71, s71, 7680\n\t" \
    "s_mul_i32 s70, s68, 640\n\t" \
    "s_add_u32 s71, s71, s70\n\t" \
    "v_add_u32 v33, s71, v9\n\t" \
    "s_and_b64 vcc, vcc, s[48:49]\n\t" \
    "v_mov_b32 v31, 0x7f000000\n\t" \
    "v_cndmask_b32 v11, v31, v33, vcc\n\t" \
    "s_cmp_lt_u32 s67, 4\n\t" \
    "s_cselect_b32 s71, 4, 0\n\t" \
    "s_cselect_b32 s70, 0, 4\n\t" \
    "v_cmp_gt_u32 vcc, s71, v32\n\t" \
    "s_and_b32 s67, s67, 3\n\t" \
    "s_lshl2_add_u32 s67, s72, s67\n\t" \
    "s_lshl2_add_u32 s67, s67, s70\n\t" \
    "s_mul_i32 s67, s67, 3840\n\t" \
    "s_mul_i32 s68, s68, 640\n\t" \
    "s_add_u32 s51, s67, s68\n\t" \
    "s_and_b64 vcc, vcc, s[48:49]\n\t" \
    "s_mov_b32 s49, 245760\n\t" \
    "v_cndmask_b32 v9, v31, v9, vcc\n\t" \
    "LPE_%=:\n\t" \
    "v_mov_b32_dpp v10, v8 quad_perm:[3,2,1,0] row_mask:0xf bank_mask:0xf\n\t" \
    "v_add_u32 v9, s51, v9\n\t" \
    "s_lshr_b32 s51, s75, 1\n\t" \
    "s_mul_i32 s48, s51, s49\n\t" \
    "s_lshl_b32 s49, s49, 1\n\t" \
    "s_mov_b32 s50, 0x7f000000\n\t" \
    "s_lshl_b32 s56, s75, 13\n\t" \
    "s_lshl_b32 s57, s75, 12\n\t" \
    "s_add_u32 s57, s57, 32768\n\t" \
    "s_mov_b32 s58, 0\n\t" \
    "s_mov_b32 s59, 0\n\t" \
    "s_mov_b32 s38, 327680\n\t" \
    "s_mov_b32 s39, 0x20000\n\t" \
    "s_mov_b32 s42, 2457600\n\t" \
    "s_mov_b32 s43, 0x20000\n\t" \
    "s_mul_i32 s71, s61, 327680\n\t" \
    "s_bitcmp1_b32 s2, 2\n\t" \
    "s_waitcnt lgkmcnt(0)\n\t" \
    "s_cselect_b64 s[36:37], s[78:79], s[76:77]\n\t" \
    "s_cselect_b64 s[40:41], s[82:83], s[80:81]\n\t" \
    "s_add_u32 s36, s36, s71\n\t" \
    "s_addc_u32 s37, s37, 0\n\t" \
    "s_and_b32 s37, s37, 0xffff\n\t" \
    "s_and_b32 s41, s41, 0xffff\n\t" \
    "s_bitcmp1_b32 s75, 0\n\t" \
    "s_cselect_b64 s[44:45], s[40:41], s[36:37]\n\t" \
    "s_cselect_b64 s[46:47], s[42:43], s[38:39]\n\t" \
    "s_mov_b32 s70, 0\n\t" \
    "buffer_load_dword v28, v11, s[44:47], s48 offen\n\t" \
    "s_add_u32 s48, s48, s49\n\t" \
    "s_add_u32 m0, s56, 0\n\t" \
    "s_add_u32 s71, s58, 0\n\t" \
    "buffer_load_dwordx4 v2, s[36:39], s71 offen offset:0 lds\n\t" \
    "s_add_u32 s71, s58, 0\n\t" \
    "buffer_load_dwordx4 v3, s[36:39], s71 offen offset:1024 lds\n\t" \
    "s_add_u32 s71, s58, 18432\n\t" \
    "buffer_load_dwordx4 v2, s[36:39], s71 offen offset:2048 lds\n\t" \
    "s_add_u32 s71, s58, 18432\n\t" \
    "buffer_load_dwordx4 v3, s[36:39], s71 offen offset:3072 lds\n\t" \
    "s_add_u32 m0, s56, 4096\n\t" \
    "s_add_u32 s71, s58, 40960\n\t" \
    "buffer_load_dwordx4 v2, s[36:39], s71 offen offset:0 lds\n\t" \
    "s_add_u32 s71, s58, 40960\n\t" \
    "buffer_load_dwordx4 v3, s[36:39], s71 offen offset:1024 lds\n\t" \
    "s_add_u32 s71, s58, 59392\n\t" \
    "buffer_load_dwordx4 v2, s[36:39], s71 offen offset:2048 lds\n\t" \
    "s_add_u32 s71, s58, 59392\n\t" \
    "buffer_load_dwordx4 v3, s[36:39], s71 offen offset:3072 lds\n\t" \
    "s_mov_b32 m0, s57\n\t" \
    "s_add_u32 s71, s59, 0\n\t" \
    "buffer_load_dwordx4 v8, s[40:43], s71 offen offset:0 lds\n\t" \
    "s_add_u32 s71, s59, 14336\n\t" \
    "buffer_load_dwordx4 v8, s[40:43], s71 offen offset:1024 lds\n\t" \
    "s_add_u32 s71, s59, 28672\n\t" \
    "buffer_load_dwordx4 v10, s[40:43], s71 offen offset:2048 lds\n\t" \
    "s_add_u32 s71, s59, 43008\n\t" \
    "buffer_load_dwordx4 v10, s[40:43], s71 offen offset:3072 lds\n\t" \
    "s_add_u32 s58, s58, 128\n\t" \
    "s_add_u32 s59, s59, 245760\n\t" \
    "s_cmp_eq_u32 s58, 1280\n\t" \
    "s_cselect_b32 s58, 0, s58\n\t" \
    "s_cselect_b32 s59, 0, s59\n\t" \
    "s_add_u32 s70, s70, 1\n\t" \
    "s_cmp_eq_u32 s70, 3\n\t" \
    "s_cselect_b32 s71, 147456, 0\n\t" \
    "s_cselect_b32 s70, 0, s70\n\t" \
    "s_sub_u32 s71, 49152, s71\n\t" \
    "s_add_u32 s56, s56, s71\n\t" \
    "s_add_u32 s57, s57, s71\n\t" \
    "s_add_u32 m0, s56, 0\n\t" \
    "s_add_u32 s71, s58, 0\n\t" \
    "buffer_load_dwordx4 v2, s[36:39], s71 offen offset:0 lds\n\t" \
    "s_add_u32 s71, s58, 0\n\t" \
    "buffer_load_dwordx4 v3, s[36:39], s71 offen offset:1024 lds\n\t" \
    "s_add_u32 s71, s58, 18432\n\t" \
    "buffer_load_dwordx4 v2, s[36:39], s71 offen offset:2048 lds\n\t" \
    "s_add_u32 s71, s58, 18432\n\t" \
    "buffer_load_dwordx4 v3, s[36:39], s71 offen offset:3072 lds\n\t" \
    "s_add_u32 m0, s56, 4096\n\t" \
    "s_add_u32 s71, s58, 40960\n\t" \
    "buffer_load_dwordx4 v2, s[36:39], s71 offen offset:0 lds\n\t" \
    "s_add_u32 s71, s58, 40960\n\t" \
    "buffer_load_dwordx4 v3, s[36:39], s71 offen offset:1024 lds\n\t" \
    "s_add_u32 s71, s58, 59392\n\t" \
    "buffer_load_dwordx4 v2, s[36:39], s71 offen offset:2048 lds\n\t" \
    "s_add_u32 s71, s58, 59392\n\t" \
    "buffer_load_dwordx4 v3, s[36:39], s71 offen offset:3072 lds\n\t" \
    "s_mov_b32 m0, s57\n\t" \
    "s_add_u32 s71, s59, 0\n\t" \
    "buffer_load_dwordx4 v8, s[40:43], s71 offen offset:0 lds\n\t" \
    "s_add_u32 s71, s59, 14336\n\t" \
    "buffer_load_dwordx4 v8, s[40:43], s71 offen offset:1024 lds\n\t" \
    "s_add_u32 s71, s59, 28672\n\t" \
    "buffer_load_dwordx4 v10, s[40:43], s71 offen offset:2048 lds\n\t" \
    "s_add_u32 s71, s59, 43008\n\t" \
    "buffer_load_dwordx4 v10, s[40:43], s71 offen offset:3072 lds\n\t" \
    "s_add_u32 s58, s58, 128\n\t" \
    "s_add_u32 s59, s59, 245760\n\t" \
    "s_cmp_eq_u32 s58, 1280\n\t" \
    "s_cselect_b32 s58, 0, s58\n\t" \
    "s_cselect_b32 s59, 0, s59\n\t" \
    "s_add_u32 s70, s70, 1\n\t" \
    "s_cmp_eq_u32 s70, 3\n\t" \
    "s_cselect_b32 s71, 147456, 0\n\t" \
    "s_cselect_b32 s70, 0, s70\n\t" \
    "s_sub_u32 s71, 49152, s71\n\t" \
    "s_add_u32 s56, s56, s71\n\t" \
    "s_add_u32 s57, s57, s71\n\t" \
    "buffer_load_dword v28, v11, s[44:47], s48 offen\n\t" \
    "s_add_u32 s48, s48, s49\n\t" \
    "s_bfe_u32 s71, s2, 0x10002\n\t" \
    "s_lshl4_add_u32 s66, s71, s66\n\t" \
    "s_lshl2_add_u32 s66, s66, s75\n\t" \
    "s_mul_i32 s67, s66, 1024\n\t" \
    "v_and_b32 v31, 7, v30\n\t" \
    "v_mul_u32_u24 v31, 128, v31\n\t" \
    "v_add_u32 v31, s67, v31\n\t" \
    "s_lshr_b32 s66, s66, 2\n\t" \
    "s_lshl_b32 s71, s71, 8\n\t" \
    "s_add_u32 s71, s71, 262144\n\t" \
    "v_mov_b32 v33, s71\n\t" \
    "s_add_u32 s64, s64, 15728640\n\t" \
    "s_addc_u32 s65, s65, 0\n\t" \
    "global_load_dwordx2 v[4:5], v33, s[64:65] sc1\n\t" \
    "global_load_dword v28, v31, s[64:65] sc1\n\t" \
    "s_waitcnt vmcnt(15)\n\t" \
    "s_barrier\n\t" \
    "s_mov_b32 s69, 0\n\t" \
    "LG_%=:\n\t" \
    "s_cmp_ge_u32 s69, 6\n\t" \
    "s_cbranch_scc1 LNP_%=\n\t" \
    "s_and_b32 s71, s69, 1\n\t" \
    "s_cmp_eq_u32 s71, s51\n\t" \
    "s_cselect_b32 s71, s48, s50\n\t" \
    "buffer_load_dword v28, v9, s[44:47], s71 offen\n\t" \
    "s_cselect_b32 s71, s49, 0\n\t" \
    "s_add_u32 s48, s48, s71\n\t" \
    "LNP_%=:\n\t" \
    "s_add_u32 m0, s56, 0\n\t" \
    "s_add_u32 s71, s58, 0\n\t" \
    "buffer_load_dwordx4 v2, s[36:39], s71 offen offset:0 lds\n\t" \
    "s_add_u32 s71, s58, 0\n\t" \
    "buffer_load_dwordx4 v3, s[36:39], s71 offen offset:1024 lds\n\t" \
    "s_add_u32 s71, s58, 18432\n\t" \
    "buffer_load_dwordx4 v2, s[36:39], s71 offen offset:2048 lds\n\t" \
    "s_add_u32 s71, s58, 18432\n\t" \
    "buffer_load_dwordx4 v3, s[36:39], s71 offen offset:3072 lds\n\t" \
    "s_add_u32 m0, s56, 4096\n\t" \
    "s_add_u32 s71, s58, 40960\n\t" \
    "buffer_load_dwordx4 v2, s[36:39], s71 offen offset:0 lds\n\t" \
    "s_add_u32 s71, s58, 40960\n\t" \
    "buffer_load_dwordx4 v3, s[36:39], s71 offen offset:1024 lds\n\t" \
    "s_add_u32 s71, s58, 59392\n\t" \
    "buffer_load_dwordx4 v2, s[36:39], s71 offen offset:2048 lds\n\t" \
    "s_add_u32 s71, s58, 59392\n\t" \
    "buffer_load_dwordx4 v3, s[36:39], s71 offen offset:3072 lds\n\t" \
    "s_mov_b32 m0, s57\n\t" \
    "s_add_u32 s71, s59, 0\n\t" \
    "buffer_load_dwordx4 v8, s[40:43], s71 offen offset:0 lds\n\t" \
    "s_add_u32 s71, s59, 14336\n\t" \
    "buffer_load_dwordx4 v8, s[40:43], s71 offen offset:1024 lds\n\t" \
    "s_add_u32 s71, s59, 28672\n\t" \
    "buffer_load_dwordx4 v10, s[40:43], s71 offen offset:2048 lds\n\t" \
    "s_add_u32 s71, s59, 43008\n\t" \
    "buffer_load_dwordx4 v10, s[40:43], s71 offen offset:3072 lds\n\t" \
    "s_add_u32 s58, s58, 128\n\t" \
    "s_add_u32 s59, s59, 245760\n\t" \
    "s_cmp_eq_u32 s58, 1280\n\t" \
    "s_cselect_b32 s58, 0, s58\n\t" \
    "s_cselect_b32 s59, 0, s59\n\t" \
    "s_add_u32 s70, s70, 1\n\t" \
    "s_cmp_eq_u32 s70, 3\n\t" \
    "s_cselect_b32 s71, 147456, 0\n\t" \
    "s_cselect_b32 s70, 0, s70\n\t" \
    "s_sub_u32 s71, 49152, s71\n\t" \
    "s_add_u32 s56, s56, s71\n\t" \
    "s_add_u32 s57, s57, s71\n\t" \
    "s_waitcnt vmcnt(12)\n\t" \
    "s_barrier\n\t" \
    "s_add_u32 s69, s69, 1\n\t" \
    "s_cmp_lt_u32 s69, 8\n\t" \
    "s_cbranch_scc1 LG_%=\n\t" \
    "s_waitcnt vmcnt(0) lgkmcnt(0)\n\t" \
    "s_barrier\n\t" \
    "LGE_%=:\n\t" \
    "s_waitcnt lgkmcnt(0)\n\t" \
    : "={v[4:5]}"(ep) \
    : "{v0}"(tid), "{s[0:1]}"(kp), "{s2}"(bid) \
    : "v2", "v3", "v8", "v10", "v9", "v11", "v28", "v30", "v31", "v32", "v33", "s36", "s37", "s38", "s39", "s40", "s41", "s42", "s43", "s44", "s45", "s46", "s47", "s48", "s49", "s50", "s51", "s56", "s57", "s58", "s59", "s60", "s61", "s64", "s65", "s66", "s67", "s68", "s69", "s70", "s71", "s72", "s73", "s74", "s75", "s76", "s77", "s78", "s79", "s80", "s81", "s82", "s83", "vcc", "scc", "memory")

#define LOADER_A(vtag, misc, kvsrd, fsrd, wosrd, sw, km, stb, foff, poff, wofr, sli, osrd, spf, sig, hoff) \
  asm volatile( \
    "s_mov_b32 s69, 0\n\t" \
    "s_mov_b32 s73, -1\n\t" \
    "s_mov_b32 s72, 200000\n\t" \
    "s_mov_b32 s74, 40960\n\t" \
    "s_and_b32 s71, s75, 1\n\t" \
    "s_lshl_b32 s71, s71, 2\n\t" \
    "s_add_u32 s71, s71, s42\n\t" \
    "s_add_u32 s71, s71, 163824\n\t" \
    "v_mov_b32 v29, s71\n\t" \
    "v_mov_b32 v6, 0\n\t" \
    "ds_write_b32 v29, v6\n\t" \
    "s_waitcnt lgkmcnt(0)\n\t" \
    "s_barrier\n\t" \
    "s_cmp_ge_u32 s75, 2\n\t" \
    "s_cbranch_scc1 LTB_%=\n\t" \
    "ds_read_b128 v[30:33], v24 offset:0\n\t" \
    "ds_read_b128 v[34:37], v24 offset:1024\n\t" \
    "ds_read_b128 v[38:41], v24 offset:2048\n\t" \
    "ds_read_b128 v[42:45], v24 offset:3072\n\t" \
    "ds_read_b128 v[46:49], v24 offset:4096\n\t" \
    "ds_read_b128 v[50:53], v24 offset:5120\n\t" \
    "ds_read_b128 v[54:57], v24 offset:6144\n\t" \
    "ds_read_b128 v[58:61], v24 offset:7168\n\t" \
    "ds_read_b128 v[62:65], v24 offset:8192\n\t" \
    "ds_read_b128 v[66:69], v24 offset:9216\n\t" \
    "s_mul_i32 s71, s61, 20480\n\t" \
    "s_waitcnt lgkmcnt(9)\n\t" \
    "buffer_store_dwordx4 v[30:33], v23, s[44:47], s71 offen offset:0 sc1\n\t" \
    "s_waitcnt lgkmcnt(8)\n\t" \
    "buffer_store_dwordx4 v[34:37], v23, s[44:47], s71 offen offset:1024 sc1\n\t" \
    "s_waitcnt lgkmcnt(7)\n\t" \
    "buffer_store_dwordx4 v[38:41], v23, s[44:47], s71 offen offset:2048 sc1\n\t" \
    "s_waitcnt lgkmcnt(6)\n\t" \
    "buffer_store_dwordx4 v[42:45], v23, s[44:47], s71 offen offset:3072 sc1\n\t" \
    "s_add_u32 s71, s71, 4096\n\t" \
    "s_waitcnt lgkmcnt(5)\n\t" \
    "buffer_store_dwordx4 v[46:49], v23, s[44:47], s71 offen offset:0 sc1\n\t" \
    "s_waitcnt lgkmcnt(4)\n\t" \
    "buffer_store_dwordx4 v[50:53], v23, s[44:47], s71 offen offset:1024 sc1\n\t" \
    "s_waitcnt lgkmcnt(3)\n\t" \
    "buffer_store_dwordx4 v[54:57], v23, s[44:47], s71 offen offset:2048 sc1\n\t" \
    "s_waitcnt lgkmcnt(2)\n\t" \
    "buffer_store_dwordx4 v[58:61], v23, s[44:47], s71 offen offset:3072 sc1\n\t" \
    "s_add_u32 s71, s71, 4096\n\t" \
    "s_waitcnt lgkmcnt(1)\n\t" \
    "buffer_store_dwordx4 v[62:65], v23, s[44:47], s71 offen offset:0 sc1\n\t" \
    "s_waitcnt lgkmcnt(0)\n\t" \
    "buffer_store_dwordx4 v[66:69], v23, s[44:47], s71 offen offset:1024 sc1\n\t" \
    "s_waitcnt vmcnt(0)\n\t" \
    "v_mov_b32 v28, 1\n\t" \
    "ds_write_b32 v29, v28\n\t" \
    "s_mov_b64 exec, 1\n\t" \
    "buffer_store_dwordx2 v[4:5], off, s[48:51], s64 sc1\n\t" \
    "s_mov_b64 exec, -1\n\t" \
    "s_add_u32 s71, s69, 1\n\t" \
    "s_bitcmp0_b32 s73, s71\n\t" \
    "s_cbranch_scc1 LAD_%=\n\t" \
    "LAP_%=:\n\t" \
    "buffer_load_dwordx2 v[6:7], v25, s[48:51], s65 offen sc1\n\t" \
    "s_cmp_eq_u32 s72, 0\n\t" \
    "s_cbranch_scc1 LAD_%=\n\t" \
    "s_sub_u32 s72, s72, 1\n\t" \
    "s_waitcnt vmcnt(0)\n\t" \
    "v_cmp_ne_u64 vcc, v[6:7], v[4:5]\n\t" \
    "s_nop 1\n\t" \
    "s_mov_b32 s73, vcc_lo\n\t" \
    "s_bitcmp1_b32 s73, s71\n\t" \
    "s_cbranch_scc1 LAP_%=\n\t" \
    "LAD_%=:\n\t" \
    "s_add_u32 s71, s69, 1\n\t" \
    "s_xor_b32 s71, s71, s61\n\t" \
    "s_mul_i32 s71, s71, 20480\n\t" \
    "s_add_u32 m0, s60, s74\n\t" \
    "s_xor_b32 s74, s74, 40960\n\t" \
    "buffer_load_dwordx4 v23, s[44:47], s71 offen offset:0 sc1 lds\n\t" \
    "buffer_load_dwordx4 v23, s[44:47], s71 offen offset:1024 sc1 lds\n\t" \
    "buffer_load_dwordx4 v23, s[44:47], s71 offen offset:2048 sc1 lds\n\t" \
    "buffer_load_dwordx4 v23, s[44:47], s71 offen offset:3072 sc1 lds\n\t" \
    "s_add_u32 m0, m0, 4096\n\t" \
    "s_add_u32 s71, s71, 4096\n\t" \
    "buffer_load_dwordx4 v23, s[44:47], s71 offen offset:0 sc1 lds\n\t" \
    "buffer_load_dwordx4 v23, s[44:47], s71 offen offset:1024 sc1 lds\n\t" \
    "buffer_load_dwordx4 v23, s[44:47], s71 offen offset:2048 sc1 lds\n\t" \
    "buffer_load_dwordx4 v23, s[44:47], s71 offen offset:3072 sc1 lds\n\t" \
    "s_add_u32 m0, m0, 4096\n\t" \
    "s_add_u32 s71, s71, 4096\n\t" \
    "buffer_load_dwordx4 v23, s[44:47], s71 offen offset:0 sc1 lds\n\t" \
    "buffer_load_dwordx4 v23, s[44:47], s71 offen offset:1024 sc1 lds\n\t" \
    "s_waitcnt vmcnt(0)\n\t" \
    "s_barrier\n\t" \
    "s_add_u32 s71, s69, 2\n\t" \
    "s_bitcmp0_b32 s73, s71\n\t" \
    "s_cbranch_scc1 LED_%=\n\t" \
    "LEP_%=:\n\t" \
    "buffer_load_dwordx2 v[6:7], v25, s[48:51], s65 offen sc1\n\t" \
    "s_cmp_eq_u32 s72, 0\n\t" \
    "s_cbranch_scc1 LED_%=\n\t" \
    "s_sub_u32 s72, s72, 1\n\t" \
    "s_waitcnt vmcnt(0)\n\t" \
    "v_cmp_ne_u64 vcc, v[6:7], v[4:5]\n\t" \
    "s_nop 1\n\t" \
    "s_mov_b32 s73, vcc_lo\n\t" \
    "s_bitcmp1_b32 s73, s71\n\t" \
    "s_cbranch_scc1 LEP_%=\n\t" \
    "LED_%=:\n\t" \
    "s_add_u32 s71, s69, 2\n\t" \
    "s_xor_b32 s71, s71, s61\n\t" \
    "s_mul_i32 s71, s71, 20480\n\t" \
    "s_add_u32 m0, s60, s74\n\t" \
    "s_xor_b32 s74, s74, 40960\n\t" \
    "buffer_load_dwordx4 v23, s[44:47], s71 offen offset:0 sc1 lds\n\t" \
    "buffer_load_dwordx4 v23, s[44:47], s71 offen offset:1024 sc1 lds\n\t" \
    "buffer_load_dwordx4 v23, s[44:47], s71 offen offset:2048 sc1 lds\n\t" \
    "buffer_load_dwordx4 v23, s[44:47], s71 offen offset:3072 sc1 lds\n\t" \
    "s_add_u32 m0, m0, 4096\n\t" \
    "s_add_u32 s71, s71, 4096\n\t" \
    "buffer_load_dwordx4 v23, s[44:47], s71 offen offset:0 sc1 lds\n\t" \
    "buffer_load_dwordx4 v23, s[44:47], s71 offen offset:1024 sc1 lds\n\t" \
    "buffer_load_dwordx4 v23, s[44:47], s71 offen offset:2048 sc1 lds\n\t" \
    "buffer_load_dwordx4 v23, s[44:47], s71 offen offset:3072 sc1 lds\n\t" \
    "s_add_u32 m0, m0, 4096\n\t" \
    "s_add_u32 s71, s71, 4096\n\t" \
    "buffer_load_dwordx4 v23, s[44:47], s71 offen offset:0 sc1 lds\n\t" \
    "buffer_load_dwordx4 v23, s[44:47], s71 offen offset:1024 sc1 lds\n\t" \
    "s_barrier\n\t" \
    "s_branch LN_%=\n\t" \
    "LTB_%=:\n\t" \
    "ds_read_b32 v6, v29\n\t" \
    "s_waitcnt lgkmcnt(0)\n\t" \
    "v_cmp_eq_u32 vcc, 0, v6\n\t" \
    "s_and_b64 vcc, exec, vcc\n\t" \
    "s_cbranch_vccz LTC_%=\n\t" \
    "s_sleep 1\n\t" \
    "s_branch LTB_%=\n\t" \
    "LTC_%=:\n\t" \
    "ds_read_b128 v[30:33], v24 offset:0\n\t" \
    "ds_read_b128 v[34:37], v24 offset:1024\n\t" \
    "ds_read_b128 v[38:41], v24 offset:2048\n\t" \
    "ds_read_b128 v[42:45], v24 offset:3072\n\t" \
    "ds_read_b128 v[46:49], v24 offset:4096\n\t" \
    "ds_read_b128 v[50:53], v24 offset:5120\n\t" \
    "ds_read_b128 v[54:57], v24 offset:6144\n\t" \
    "ds_read_b128 v[58:61], v24 offset:7168\n\t" \
    "ds_read_b128 v[62:65], v24 offset:8192\n\t" \
    "ds_read_b128 v[66:69], v24 offset:9216\n\t" \
    "s_mul_i32 s71, s61, 20480\n\t" \
    "s_waitcnt lgkmcnt(9)\n\t" \
    "buffer_store_dwordx4 v[30:33], v23, s[44:47], s71 offen offset:0 sc1\n\t" \
    "s_waitcnt lgkmcnt(8)\n\t" \
    "buffer_store_dwordx4 v[34:37], v23, s[44:47], s71 offen offset:1024 sc1\n\t" \
    "s_waitcnt lgkmcnt(7)\n\t" \
    "buffer_store_dwordx4 v[38:41], v23, s[44:47], s71 offen offset:2048 sc1\n\t" \
    "s_waitcnt lgkmcnt(6)\n\t" \
    "buffer_store_dwordx4 v[42:45], v23, s[44:47], s71 offen offset:3072 sc1\n\t" \
    "s_add_u32 s71, s71, 4096\n\t" \
    "s_waitcnt lgkmcnt(5)\n\t" \
    "buffer_store_dwordx4 v[46:49], v23, s[44:47], s71 offen offset:0 sc1\n\t" \
    "s_waitcnt lgkmcnt(4)\n\t" \
    "buffer_store_dwordx4 v[50:53], v23, s[44:47], s71 offen offset:1024 sc1\n\t" \
    "s_waitcnt lgkmcnt(3)\n\t" \
    "buffer_store_dwordx4 v[54:57], v23, s[44:47], s71 offen offset:2048 sc1\n\t" \
    "s_waitcnt lgkmcnt(2)\n\t" \
    "buffer_store_dwordx4 v[58:61], v23, s[44:47], s71 offen offset:3072 sc1\n\t" \
    "s_add_u32 s71, s71, 4096\n\t" \
    "s_waitcnt lgkmcnt(1)\n\t" \
    "buffer_store_dwordx4 v[62:65], v23, s[44:47], s71 offen offset:0 sc1\n\t" \
    "s_waitcnt lgkmcnt(0)\n\t" \
    "buffer_store_dwordx4 v[66:69], v23, s[44:47], s71 offen offset:1024 sc1\n\t" \
    "s_barrier\n\t" \
    "s_waitcnt vmcnt(0)\n\t" \
    "s_mov_b64 exec, 1\n\t" \
    "buffer_store_dwordx2 v[4:5], off, s[48:51], s64 sc1\n\t" \
    "s_mov_b64 exec, -1\n\t" \
    "s_add_u32 s71, s69, 1\n\t" \
    "s_bitcmp0_b32 s73, s71\n\t" \
    "s_cbranch_scc1 LQD_%=\n\t" \
    "LQP_%=:\n\t" \
    "buffer_load_dwordx2 v[6:7], v25, s[48:51], s65 offen sc1\n\t" \
    "s_cmp_eq_u32 s72, 0\n\t" \
    "s_cbranch_scc1 LQD_%=\n\t" \
    "s_sub_u32 s72, s72, 1\n\t" \
    "s_waitcnt vmcnt(0)\n\t" \
    "v_cmp_ne_u64 vcc, v[6:7], v[4:5]\n\t" \
    "s_nop 1\n\t" \
    "s_mov_b32 s73, vcc_lo\n\t" \
    "s_bitcmp1_b32 s73, s71\n\t" \
    "s_cbranch_scc1 LQP_%=\n\t" \
    "LQD_%=:\n\t" \
    "s_add_u32 s71, s69, 1\n\t" \
    "s_xor_b32 s71, s71, s61\n\t" \
    "s_mul_i32 s71, s71, 20480\n\t" \
    "s_add_u32 m0, s60, s74\n\t" \
    "s_xor_b32 s74, s74, 40960\n\t" \
    "buffer_load_dwordx4 v23, s[44:47], s71 offen offset:0 sc1 lds\n\t" \
    "buffer_load_dwordx4 v23, s[44:47], s71 offen offset:1024 sc1 lds\n\t" \
    "buffer_load_dwordx4 v23, s[44:47], s71 offen offset:2048 sc1 lds\n\t" \
    "buffer_load_dwordx4 v23, s[44:47], s71 offen offset:3072 sc1 lds\n\t" \
    "s_add_u32 m0, m0, 4096\n\t" \
    "s_add_u32 s71, s71, 4096\n\t" \
    "buffer_load_dwordx4 v23, s[44:47], s71 offen offset:0 sc1 lds\n\t" \
    "buffer_load_dwordx4 v23, s[44:47], s71 offen offset:1024 sc1 lds\n\t" \
    "buffer_load_dwordx4 v23, s[44:47], s71 offen offset:2048 sc1 lds\n\t" \
    "buffer_load_dwordx4 v23, s[44:47], s71 offen offset:3072 sc1 lds\n\t" \
    "s_add_u32 m0, m0, 4096\n\t" \
    "s_add_u32 s71, s71, 4096\n\t" \
    "buffer_load_dwordx4 v23, s[44:47], s71 offen offset:0 sc1 lds\n\t" \
    "buffer_load_dwordx4 v23, s[44:47], s71 offen offset:1024 sc1 lds\n\t" \
    "s_waitcnt vmcnt(0)\n\t" \
    "s_barrier\n\t" \
    "s_add_u32 s71, s69, 2\n\t" \
    "s_bitcmp0_b32 s73, s71\n\t" \
    "s_cbranch_scc1 LRD_%=\n\t" \
    "LRP_%=:\n\t" \
    "buffer_load_dwordx2 v[6:7], v25, s[48:51], s65 offen sc1\n\t" \
    "s_cmp_eq_u32 s72, 0\n\t" \
    "s_cbranch_scc1 LRD_%=\n\t" \
    "s_sub_u32 s72, s72, 1\n\t" \
    "s_waitcnt vmcnt(0)\n\t" \
    "v_cmp_ne_u64 vcc, v[6:7], v[4:5]\n\t" \
    "s_nop 1\n\t" \
    "s_mov_b32 s73, vcc_lo\n\t" \
    "s_bitcmp1_b32 s73, s71\n\t" \
    "s_cbranch_scc1 LRP_%=\n\t" \
    "LRD_%=:\n\t" \
    "s_add_u32 s71, s69, 2\n\t" \
    "s_xor_b32 s71, s71, s61\n\t" \
    "s_mul_i32 s71, s71, 20480\n\t" \
    "s_add_u32 m0, s60, s74\n\t" \
    "s_xor_b32 s74, s74, 40960\n\t" \
    "buffer_load_dwordx4 v23, s[44:47], s71 offen offset:0 sc1 lds\n\t" \
    "buffer_load_dwordx4 v23, s[44:47], s71 offen offset:1024 sc1 lds\n\t" \
    "buffer_load_dwordx4 v23, s[44:47], s71 offen offset:2048 sc1 lds\n\t" \
    "buffer_load_dwordx4 v23, s[44:47], s71 offen offset:3072 sc1 lds\n\t" \
    "s_add_u32 m0, m0, 4096\n\t" \
    "s_add_u32 s71, s71, 4096\n\t" \
    "buffer_load_dwordx4 v23, s[44:47], s71 offen offset:0 sc1 lds\n\t" \
    "buffer_load_dwordx4 v23, s[44:47], s71 offen offset:1024 sc1 lds\n\t" \
    "buffer_load_dwordx4 v23, s[44:47], s71 offen offset:2048 sc1 lds\n\t" \
    "buffer_load_dwordx4 v23, s[44:47], s71 offen offset:3072 sc1 lds\n\t" \
    "s_add_u32 m0, m0, 4096\n\t" \
    "s_add_u32 s71, s71, 4096\n\t" \
    "buffer_load_dwordx4 v23, s[44:47], s71 offen offset:0 sc1 lds\n\t" \
    "buffer_load_dwordx4 v23, s[44:47], s71 offen offset:1024 sc1 lds\n\t" \
    "s_branch LN_%=\n\t" \
    "LC_%=:\n\t" \
    "s_add_u32 s71, s69, 2\n\t" \
    "s_bitcmp0_b32 s73, s71\n\t" \
    "s_cbranch_scc1 LBD_%=\n\t" \
    "LBP_%=:\n\t" \
    "buffer_load_dwordx2 v[6:7], v25, s[48:51], s65 offen sc1\n\t" \
    "s_cmp_eq_u32 s72, 0\n\t" \
    "s_cbranch_scc1 LBD_%=\n\t" \
    "s_sub_u32 s72, s72, 1\n\t" \
    "s_waitcnt vmcnt(0)\n\t" \
    "v_cmp_ne_u64 vcc, v[6:7], v[4:5]\n\t" \
    "s_nop 1\n\t" \
    "s_mov_b32 s73, vcc_lo\n\t" \
    "s_bitcmp1_b32 s73, s71\n\t" \
    "s_cbranch_scc1 LBP_%=\n\t" \
    "LBD_%=:\n\t" \
    "s_add_u32 s71, s69, 2\n\t" \
    "s_xor_b32 s71, s71, s61\n\t" \
    "s_mul_i32 s71, s71, 20480\n\t" \
    "s_add_u32 m0, s60, s74\n\t" \
    "s_xor_b32 s74, s74, 40960\n\t" \
    "buffer_load_dwordx4 v23, s[44:47], s71 offen offset:0 sc1 lds\n\t" \
    "buffer_load_dwordx4 v23, s[44:47], s71 offen offset:1024 sc1 lds\n\t" \
    "buffer_load_dwordx4 v23, s[44:47], s71 offen offset:2048 sc1 lds\n\t" \
    "buffer_load_dwordx4 v23, s[44:47], s71 offen offset:3072 sc1 lds\n\t" \
    "s_add_u32 m0, m0, 4096\n\t" \
    "s_add_u32 s71, s71, 4096\n\t" \
    "buffer_load_dwordx4 v23, s[44:47], s71 offen offset:0 sc1 lds\n\t" \
    "buffer_load_dwordx4 v23, s[44:47], s71 offen offset:1024 sc1 lds\n\t" \
    "buffer_load_dwordx4 v23, s[44:47], s71 offen offset:2048 sc1 lds\n\t" \
    "buffer_load_dwordx4 v23, s[44:47], s71 offen offset:3072 sc1 lds\n\t" \
    "s_add_u32 m0, m0, 4096\n\t" \
    "s_add_u32 s71, s71, 4096\n\t" \
    "buffer_load_dwordx4 v23, s[44:47], s71 offen offset:0 sc1 lds\n\t" \
    "buffer_load_dwordx4 v23, s[44:47], s71 offen offset:1024 sc1 lds\n\t" \
    "s_cmp_eq_u32 s69, 1\n\t" \
    "s_cbranch_scc0 LF1_%=\n\t" \
    "v_mov_b32 v30, -1\n\t" \
    "v_mov_b32 v31, -1\n\t" \
    "v_mov_b32 v32, -1\n\t" \
    "v_mov_b32 v33, -1\n\t" \
    "s_mov_b32 s71, 0\n\t" \
    "buffer_store_dwordx4 v[30:33], v22, s[36:39], s71 offen sc1\n\t" \
    "s_mov_b32 s71, 20480\n\t" \
    "buffer_store_dwordx4 v[30:33], v22, s[36:39], s71 offen sc1\n\t" \
    "s_mov_b32 s71, 40960\n\t" \
    "buffer_store_dwordx4 v[30:33], v22, s[36:39], s71 offen sc1\n\t" \
    "s_mov_b32 s71, 61440\n\t" \
    "buffer_store_dwordx4 v[30:33], v22, s[36:39], s71 offen sc1\n\t" \
    "buffer_store_dwordx4 v[30:33], v26, s[36:39], 0 offen sc1\n\t" \
    "s_mov_b64 exec, 1\n\t" \
    "buffer_store_dword v30, off, s[48:51], s43 sc1\n\t" \
    "s_mov_b64 exec, -1\n\t" \
    "LF1_%=:\n\t" \
    "s_cmp_eq_u32 s69, 2\n\t" \
    "s_cbranch_scc0 LF2_%=\n\t" \
    "s_mov_b64 exec, 1\n\t" \
    "buffer_store_dwordx2 v[4:5], off, s[48:51], s40 sc1\n\t" \
    "s_mov_b64 exec, -1\n\t" \
    "LF2_%=:\n\t" \
    "s_cmp_eq_u32 s69, 5\n\t" \
    "s_cbranch_scc0 LF3_%=\n\t" \
    "LFP_%=:\n\t" \
    "buffer_load_dwordx2 v[6:7], v27, s[48:51], s41 offen sc1\n\t" \
    "s_cmp_eq_u32 s72, 0\n\t" \
    "s_cbranch_scc1 LF3_%=\n\t" \
    "s_sub_u32 s72, s72, 1\n\t" \
    "s_waitcnt vmcnt(0)\n\t" \
    "v_cmp_ne_u64 vcc, v[6:7], v[4:5]\n\t" \
    "s_and_b64 vcc, exec, vcc\n\t" \
    "s_cbranch_vccnz LFP_%=\n\t" \
    "LF3_%=:\n\t" \
    "s_bitcmp0_b32 30, s69\n\t" \
    "s_cbranch_scc1 LN_%=\n\t" \
    "s_mov_b32 m0, s66\n\t" \
    "s_nop 0\n\t" \
    "buffer_load_dwordx4 v12, s[52:55], s67 offen offset:0 lds\n\t" \
    "buffer_load_dwordx4 v13, s[52:55], s67 offen offset:1024 lds\n\t" \
    "buffer_load_dwordx4 v14, s[52:55], s67 offen offset:2048 lds\n\t" \
    "buffer_load_dwordx4 v15, s[52:55], s67 offen offset:3072 lds\n\t" \
    "s_add_u32 m0, m0, 4096\n\t" \
    "s_nop 0\n\t" \
    "buffer_load_dwordx4 v16, s[52:55], s67 offen lds\n\t" \
    "s_add_u32 s66, s66, 20480\n\t" \
    "s_add_u32 s67, s67, 204800\n\t" \
    "LN_%=:\n\t" \
    "s_waitcnt vmcnt(0)\n\t" \
    "s_barrier\n\t" \
    "s_add_u32 s69, s69, 1\n\t" \
    "s_cmp_lt_u32 s69, 6\n\t" \
    "s_cbranch_scc1 LC_%=\n\t" \
    "v_mov_b32 v62, 0\n\t" \
    "v_mov_b32 v63, 0\n\t" \
    "v_mov_b32 v64, v4\n\t" \
    "v_mov_b32 v65, v5\n\t" \
    "s_add_u32 s71, s42, 102400\n\t" \
    "v_mov_b32 v29, s71\n\t" \
    "ds_write_b128 v29, v[62:65]\n\t" \
    "s_mov_b32 m0, s66\n\t" \
    "s_nop 0\n\t" \
    "buffer_load_dwordx4 v17, s[52:55], s68 offen offset:0 lds\n\t" \
    "buffer_load_dwordx4 v18, s[52:55], s68 offen offset:1024 lds\n\t" \
    "buffer_load_dwordx4 v19, s[52:55], s68 offen offset:2048 lds\n\t" \
    "buffer_load_dwordx4 v20, s[52:55], s68 offen offset:3072 lds\n\t" \
    "s_add_u32 m0, m0, 4096\n\t" \
    "s_nop 0\n\t" \
    "buffer_load_dwordx4 v21, s[52:55], s68 offen lds\n\t" \
    "s_add_u32 s66, s66, 20480\n\t" \
    "s_add_u32 s67, s67, 204800\n\t" \
    "s_waitcnt vmcnt(0) lgkmcnt(0)\n\t" \
    "s_barrier\n\t" \
    : "+{s[66:67]}"(sw) \
    : "{v[4:5]}"(vtag), "{v[12:27]}"(misc), "{s[44:47]}"(kvsrd), "{s[48:51]}"(fsrd), "{s[52:55]}"(wosrd), "{s60}"(km), "{s61}"(stb), "{s64}"(foff), "{s65}"(poff), "{s68}"(wofr), "{s75}"(sli), "{s[36:39]}"(osrd), "{s[40:41]}"(spf), "{s42}"(sig), "{s43}"(hoff) \
    : "v6", "v7", "v29", "v28", "v30", "v31", "v32", "v33", "v34", "v35", "v36", "v37", "v38", "v39", "v40", "v41", "v42", "v43", "v44", "v45", "v46", "v47", "v48", "v49", "v50", "v51", "v52", "v53", "v54", "v55", "v56", "v57", "v58", "v59", "v60", "v61", "v62", "v63", "v64", "v65", "v66", "v67", "v68", "v69", "s69", "s71", "s72", "s73", "s74", "vcc", "scc", "memory")

#define GEMM_CORE(o, xa, wa, qm, qr, km0, kr0, cr, akw, avw, sc, vmask, lomask, ost, aK0, aV0, ones, vP, osrd, hw, wsrd) \
  asm volatile( \
    "v_mov_b32 v40, 0\n\t" \
    "v_mov_b32 v41, 0\n\t" \
    "v_mov_b32 v42, 0\n\t" \
    "v_mov_b32 v43, 0\n\t" \
    "v_mov_b32 v44, 0\n\t" \
    "v_mov_b32 v45, 0\n\t" \
    "v_mov_b32 v46, 0\n\t" \
    "v_mov_b32 v47, 0\n\t" \
    "v_mov_b32 v48, 0\n\t" \
    "v_mov_b32 v49, 0\n\t" \
    "v_mov_b32 v50, 0\n\t" \
    "v_mov_b32 v51, 0\n\t" \
    "v_mov_b32 v52, 0\n\t" \
    "v_mov_b32 v53, 0\n\t" \
    "v_mov_b32 v54, 0\n\t" \
    "v_mov_b32 v55, 0\n\t" \
    "v_mov_b32 v56, 0\n\t" \
    "v_mov_b32 v57, 0\n\t" \
    "v_mov_b32 v58, 0\n\t" \
    "v_mov_b32 v59, 0\n\t" \
    "v_mov_b32 v60, 0\n\t" \
    "v_mov_b32 v61, 0\n\t" \
    "v_mov_b32 v62, 0\n\t" \
    "v_mov_b32 v63, 0\n\t" \
    "v_mov_b32 v64, 0\n\t" \
    "v_mov_b32 v65, 0\n\t" \
    "v_mov_b32 v66, 0\n\t" \
    "v_mov_b32 v67, 0\n\t" \
    "v_mov_b32 v68, 0\n\t" \
    "v_mov_b32 v69, 0\n\t" \
    "v_mov_b32 v70, 0\n\t" \
    "v_mov_b32 v71, 0\n\t" \
    "v_mov_b32 v72, 0\n\t" \
    "v_mov_b32 v73, 0\n\t" \
    "v_mov_b32 v74, 0\n\t" \
    "v_mov_b32 v75, 0\n\t" \
    "v_mov_b32 v76, 0\n\t" \
    "v_mov_b32 v77, 0\n\t" \
    "v_mov_b32 v78, 0\n\t" \
    "v_mov_b32 v79, 0\n\t" \
    "v_mov_b32 v80, 0\n\t" \
    "v_mov_b32 v81, 0\n\t" \
    "v_mov_b32 v82, 0\n\t" \
    "v_mov_b32 v83, 0\n\t" \
    "v_mov_b32 v84, 0\n\t" \
    "v_mov_b32 v85, 0\n\t" \
    "v_mov_b32 v86, 0\n\t" \
    "v_mov_b32 v87, 0\n\t" \
    "v_mov_b32 v88, 0\n\t" \
    "v_mov_b32 v89, 0\n\t" \
    "v_mov_b32 v90, 0\n\t" \
    "v_mov_b32 v91, 0\n\t" \
    "v_mov_b32 v92, 0\n\t" \
    "v_mov_b32 v93, 0\n\t" \
    "v_mov_b32 v94, 0\n\t" \
    "v_mov_b32 v95, 0\n\t" \
    "v_mov_b32 v96, 0\n\t" \
    "v_mov_b32 v97, 0\n\t" \
    "v_mov_b32 v98, 0\n\t" \
    "v_mov_b32 v99, 0\n\t" \
    "v_mov_b32 v100, 0\n\t" \
    "v_mov_b32 v101, 0\n\t" \
    "v_mov_b32 v102, 0\n\t" \
    "v_mov_b32 v103, 0\n\t" \
    "v_mov_b32 v104, 0\n\t" \
    "v_mov_b32 v105, 0\n\t" \
    "v_mov_b32 v106, 0\n\t" \
    "v_mov_b32 v107, 0\n\t" \
    "v_mov_b32 v108, 0\n\t" \
    "v_mov_b32 v109, 0\n\t" \
    "v_mov_b32 v110, 0\n\t" \
    "v_mov_b32 v111, 0\n\t" \
    "v_mov_b32 v112, 0\n\t" \
    "v_mov_b32 v113, 0\n\t" \
    "v_mov_b32 v114, 0\n\t" \
    "v_mov_b32 v115, 0\n\t" \
    "v_mov_b32 v116, 0\n\t" \
    "v_mov_b32 v117, 0\n\t" \
    "v_mov_b32 v118, 0\n\t" \
    "v_mov_b32 v119, 0\n\t" \
    "v_mov_b32 v120, 0\n\t" \
    "v_mov_b32 v121, 0\n\t" \
    "v_mov_b32 v122, 0\n\t" \
    "v_mov_b32 v123, 0\n\t" \
    "v_mov_b32 v124, 0\n\t" \
    "v_mov_b32 v125, 0\n\t" \
    "v_mov_b32 v126, 0\n\t" \
    "v_mov_b32 v127, 0\n\t" \
    "v_mov_b32 v128, 0\n\t" \
    "v_mov_b32 v129, 0\n\t" \
    "v_mov_b32 v130, 0\n\t" \
    "v_mov_b32 v131, 0\n\t" \
    "v_mov_b32 v132, 0\n\t" \
    "v_mov_b32 v133, 0\n\t" \
    "v_mov_b32 v134, 0\n\t" \
    "v_mov_b32 v135, 0\n\t" \
    "s_mov_b32 s46, 0\n\t" \
    "s_mov_b32 s47, 0\n\t" \
    "s_barrier\n\t" \
    "ds_read_b64_tr_b16 v[34:35], v3 offset:0\n\t" \
    "ds_read_b64_tr_b16 v[36:37], v3 offset:1024\n\t" \
    "ds_read_b128 v[6:9], v4 offset:0\n\t" \
    "ds_read_b64_tr_b16 v[30:31], v2 offset:0\n\t" \
    "ds_read_b64_tr_b16 v[32:33], v2 offset:1024\n\t" \
    "ds_read_b128 v[10:13], v4 offset:2048\n\t" \
    "ds_read_b128 v[14:17], v4 offset:4096\n\t" \
    "ds_read_b128 v[18:21], v4 offset:6144\n\t" \
    "GLOOP_%=:\n\t" \
    "s_waitcnt lgkmcnt(2)\n\t" \
    "v_mfma_f32_16x16x32_bf16 v[88:91], v[34:37], v[6:9], v[88:91]\n\t" \
    "ds_read_b64_tr_b16 v[26:27], v1 offset:0\n\t" \
    "ds_read_b64_tr_b16 v[28:29], v1 offset:1024\n\t" \
    "v_mfma_f32_16x16x32_bf16 v[72:75], v[30:33], v[6:9], v[72:75]\n\t" \
    "v_mfma_f32_16x16x32_bf16 v[92:95], v[34:37], v[10:13], v[92:95]\n\t" \
    "ds_read_b64_tr_b16 v[22:23], v0 offset:0\n\t" \
    "ds_read_b64_tr_b16 v[24:25], v0 offset:1024\n\t" \
    "v_mfma_f32_16x16x32_bf16 v[76:79], v[30:33], v[10:13], v[76:79]\n\t" \
    "s_waitcnt lgkmcnt(5)\n\t" \
    "v_mfma_f32_16x16x32_bf16 v[96:99], v[34:37], v[14:17], v[96:99]\n\t" \
    "v_mfma_f32_16x16x32_bf16 v[80:83], v[30:33], v[14:17], v[80:83]\n\t" \
    "s_waitcnt lgkmcnt(4)\n\t" \
    "v_mfma_f32_16x16x32_bf16 v[100:103], v[34:37], v[18:21], v[100:103]\n\t" \
    "v_mfma_f32_16x16x32_bf16 v[84:87], v[30:33], v[18:21], v[84:87]\n\t" \
    "s_add_u32 s47, s47, 1\n\t" \
    "s_cmp_eq_u32 s47, 3\n\t" \
    "s_cselect_b32 s48, 147456, 0\n\t" \
    "s_cselect_b32 s47, 0, s47\n\t" \
    "s_sub_u32 s48, 49152, s48\n\t" \
    "v_add_u32 v4, s48, v4\n\t" \
    "s_waitcnt lgkmcnt(0)\n\t" \
    "v_mfma_f32_16x16x32_bf16 v[56:59], v[26:29], v[6:9], v[56:59]\n\t" \
    "ds_read_b64_tr_b16 v[34:35], v3 offset:8192\n\t" \
    "ds_read_b64_tr_b16 v[36:37], v3 offset:9216\n\t" \
    "v_mfma_f32_16x16x32_bf16 v[40:43], v[22:25], v[6:9], v[40:43]\n\t" \
    "ds_read_b128 v[6:9], v5 offset:0\n\t" \
    "v_mfma_f32_16x16x32_bf16 v[60:63], v[26:29], v[10:13], v[60:63]\n\t" \
    "ds_read_b64_tr_b16 v[30:31], v2 offset:8192\n\t" \
    "ds_read_b64_tr_b16 v[32:33], v2 offset:9216\n\t" \
    "v_mfma_f32_16x16x32_bf16 v[44:47], v[22:25], v[10:13], v[44:47]\n\t" \
    "ds_read_b128 v[10:13], v5 offset:2048\n\t" \
    "v_mfma_f32_16x16x32_bf16 v[64:67], v[26:29], v[14:17], v[64:67]\n\t" \
    "v_mfma_f32_16x16x32_bf16 v[48:51], v[22:25], v[14:17], v[48:51]\n\t" \
    "ds_read_b128 v[14:17], v5 offset:4096\n\t" \
    "v_mfma_f32_16x16x32_bf16 v[68:71], v[26:29], v[18:21], v[68:71]\n\t" \
    "v_mfma_f32_16x16x32_bf16 v[52:55], v[22:25], v[18:21], v[52:55]\n\t" \
    "ds_read_b128 v[18:21], v5 offset:6144\n\t" \
    "v_add_u32 v5, s48, v5\n\t" \
    "v_add_u32 v3, s48, v3\n\t" \
    "v_add_u32 v2, s48, v2\n\t" \
    "s_waitcnt lgkmcnt(2)\n\t" \
    "v_mfma_f32_16x16x32_bf16 v[88:91], v[34:37], v[6:9], v[88:91]\n\t" \
    "ds_read_b64_tr_b16 v[26:27], v1 offset:8192\n\t" \
    "ds_read_b64_tr_b16 v[28:29], v1 offset:9216\n\t" \
    "v_mfma_f32_16x16x32_bf16 v[72:75], v[30:33], v[6:9], v[72:75]\n\t" \
    "v_mfma_f32_16x16x32_bf16 v[92:95], v[34:37], v[10:13], v[92:95]\n\t" \
    "ds_read_b64_tr_b16 v[22:23], v0 offset:8192\n\t" \
    "ds_read_b64_tr_b16 v[24:25], v0 offset:9216\n\t" \
    "v_mfma_f32_16x16x32_bf16 v[76:79], v[30:33], v[10:13], v[76:79]\n\t" \
    "s_waitcnt lgkmcnt(5)\n\t" \
    "v_mfma_f32_16x16x32_bf16 v[96:99], v[34:37], v[14:17], v[96:99]\n\t" \
    "v_mfma_f32_16x16x32_bf16 v[80:83], v[30:33], v[14:17], v[80:83]\n\t" \
    "s_waitcnt lgkmcnt(4)\n\t" \
    "v_mfma_f32_16x16x32_bf16 v[100:103], v[34:37], v[18:21], v[100:103]\n\t" \
    "v_mfma_f32_16x16x32_bf16 v[84:87], v[30:33], v[18:21], v[84:87]\n\t" \
    "v_add_u32 v1, s48, v1\n\t" \
    "v_add_u32 v0, s48, v0\n\t" \
    "s_add_u32 s46, s46, 1\n\t" \
    "s_cmp_lt_u32 s46, 10\n\t" \
    "s_waitcnt lgkmcnt(0)\n\t" \
    "v_mfma_f32_16x16x32_bf16 v[56:59], v[26:29], v[6:9], v[56:59]\n\t" \
    "v_mfma_f32_16x16x32_bf16 v[40:43], v[22:25], v[6:9], v[40:43]\n\t" \
    "s_cbranch_scc0 GEND_%=\n\t" \
    "s_barrier\n\t" \
    "ds_read_b64_tr_b16 v[34:35], v3 offset:0\n\t" \
    "ds_read_b64_tr_b16 v[36:37], v3 offset:1024\n\t" \
    "ds_read_b128 v[6:9], v4 offset:0\n\t" \
    "v_mfma_f32_16x16x32_bf16 v[60:63], v[26:29], v[10:13], v[60:63]\n\t" \
    "ds_read_b64_tr_b16 v[30:31], v2 offset:0\n\t" \
    "ds_read_b64_tr_b16 v[32:33], v2 offset:1024\n\t" \
    "v_mfma_f32_16x16x32_bf16 v[44:47], v[22:25], v[10:13], v[44:47]\n\t" \
    "ds_read_b128 v[10:13], v4 offset:2048\n\t" \
    "v_mfma_f32_16x16x32_bf16 v[64:67], v[26:29], v[14:17], v[64:67]\n\t" \
    "v_mfma_f32_16x16x32_bf16 v[48:51], v[22:25], v[14:17], v[48:51]\n\t" \
    "ds_read_b128 v[14:17], v4 offset:4096\n\t" \
    "v_mfma_f32_16x16x32_bf16 v[68:71], v[26:29], v[18:21], v[68:71]\n\t" \
    "v_mfma_f32_16x16x32_bf16 v[52:55], v[22:25], v[18:21], v[52:55]\n\t" \
    "ds_read_b128 v[18:21], v4 offset:6144\n\t" \
    "s_branch GLOOP_%=\n\t" \
    "GEND_%=:\n\t" \
    "v_mfma_f32_16x16x32_bf16 v[60:63], v[26:29], v[10:13], v[60:63]\n\t" \
    "v_mfma_f32_16x16x32_bf16 v[44:47], v[22:25], v[10:13], v[44:47]\n\t" \
    "v_mfma_f32_16x16x32_bf16 v[64:67], v[26:29], v[14:17], v[64:67]\n\t" \
    "v_mfma_f32_16x16x32_bf16 v[48:51], v[22:25], v[14:17], v[48:51]\n\t" \
    "v_mfma_f32_16x16x32_bf16 v[68:71], v[26:29], v[18:21], v[68:71]\n\t" \
    "v_mfma_f32_16x16x32_bf16 v[52:55], v[22:25], v[18:21], v[52:55]\n\t" \
    "v_cvt_pk_bf16_f32 v72, v72, v73\n\t" \
    "v_cvt_pk_bf16_f32 v73, v74, v75\n\t" \
    "v_cvt_pk_bf16_f32 v74, v76, v77\n\t" \
    "v_cvt_pk_bf16_f32 v75, v78, v79\n\t" \
    "v_cvt_pk_bf16_f32 v80, v80, v81\n\t" \
    "v_cvt_pk_bf16_f32 v81, v82, v83\n\t" \
    "v_cvt_pk_bf16_f32 v82, v84, v85\n\t" \
    "v_cvt_pk_bf16_f32 v83, v86, v87\n\t" \
    "v_mul_f32 v88, v158, v88\n\t" \
    "v_mul_f32 v89, v158, v89\n\t" \
    "v_mul_f32 v90, v158, v90\n\t" \
    "v_mul_f32 v91, v158, v91\n\t" \
    "v_mul_f32 v92, v158, v92\n\t" \
    "v_mul_f32 v93, v158, v93\n\t" \
    "v_mul_f32 v94, v158, v94\n\t" \
    "v_mul_f32 v95, v158, v95\n\t" \
    "v_mul_f32 v96, v158, v96\n\t" \
    "v_mul_f32 v97, v158, v97\n\t" \
    "v_mul_f32 v98, v158, v98\n\t" \
    "v_mul_f32 v99, v158, v99\n\t" \
    "v_mul_f32 v100, v158, v100\n\t" \
    "v_mul_f32 v101, v158, v101\n\t" \
    "v_mul_f32 v102, v158, v102\n\t" \
    "v_mul_f32 v103, v158, v103\n\t" \
    "v_permlane16_swap_b32 v72, v74\n\t" \
    "v_permlane16_swap_b32 v73, v75\n\t" \
    "v_permlane16_swap_b32 v80, v82\n\t" \
    "v_permlane16_swap_b32 v81, v83\n\t" \
    "v_cvt_pk_bf16_f32 v88, v88, v89\n\t" \
    "v_cvt_pk_bf16_f32 v89, v90, v91\n\t" \
    "v_cvt_pk_bf16_f32 v90, v92, v93\n\t" \
    "v_cvt_pk_bf16_f32 v91, v94, v95\n\t" \
    "v_cvt_pk_bf16_f32 v92, v100, v101\n\t" \
    "v_cvt_pk_bf16_f32 v93, v102, v103\n\t" \
    "v_cvt_pk_bf16_f32 v94, v96, v97\n\t" \
    "v_cvt_pk_bf16_f32 v95, v98, v99\n\t" \
    "v_mfma_f32_32x32x16_bf16 v[6:21], v[72:75], v[136:139], 0\n\t" \
    "v_permlane16_swap_b32 v88, v90\n\t" \
    "v_permlane16_swap_b32 v89, v91\n\t" \
    "v_permlane16_swap_b32 v94, v92\n\t" \
    "v_permlane16_swap_b32 v95, v93\n\t" \
    "v_mfma_f32_32x32x16_bf16 v[22:37], v[80:83], v[136:139], 0\n\t" \
    "v_cndmask_b32 v144, 0, v88, s[52:53]\n\t" \
    "v_cndmask_b32 v145, 0, v89, s[52:53]\n\t" \
    "v_cndmask_b32 v88, v88, 0, s[52:53]\n\t" \
    "v_cndmask_b32 v89, v89, 0, s[52:53]\n\t" \
    "v_cndmask_b32 v146, 0, v94, s[52:53]\n\t" \
    "v_cndmask_b32 v147, 0, v95, s[52:53]\n\t" \
    "v_cndmask_b32 v94, v94, 0, s[52:53]\n\t" \
    "v_cndmask_b32 v95, v95, 0, s[52:53]\n\t" \
    "v_cvt_pk_bf16_f32 v56, v56, v57\n\t" \
    "v_cvt_pk_bf16_f32 v57, v58, v59\n\t" \
    "v_cvt_pk_bf16_f32 v58, v60, v61\n\t" \
    "v_cvt_pk_bf16_f32 v59, v62, v63\n\t" \
    "v_mfma_f32_32x32x8_bf16 v[6:21], v[88:89], v[140:141], v[6:21]\n\t" \
    "v_cvt_pk_bf16_f32 v64, v64, v65\n\t" \
    "v_cvt_pk_bf16_f32 v65, v66, v67\n\t" \
    "v_cvt_pk_bf16_f32 v66, v68, v69\n\t" \
    "v_cvt_pk_bf16_f32 v67, v70, v71\n\t" \
    "v_mfma_f32_32x32x8_bf16 v[22:37], v[94:95], v[140:141], v[22:37]\n\t" \
    "v_permlane16_swap_b32 v56, v58\n\t" \
    "v_permlane16_swap_b32 v57, v59\n\t" \
    "v_permlane16_swap_b32 v64, v66\n\t" \
    "v_permlane16_swap_b32 v65, v67\n\t" \
    "ds_write_b128 v159, v[56:59] offset:0\n\t" \
    "ds_write_b128 v159, v[64:67] offset:1024\n\t" \
    "s_mov_b64 exec, s[52:53]\n\t" \
    "ds_write_b128 v159, v[90:93] offset:2048\n\t" \
    "s_mov_b64 exec, -1\n\t" \
    "v_cvt_pk_bf16_f32 v6, v6, v7\n\t" \
    "v_cvt_pk_bf16_f32 v7, v8, v9\n\t" \
    "v_cvt_pk_bf16_f32 v8, v10, v11\n\t" \
    "v_cvt_pk_bf16_f32 v9, v12, v13\n\t" \
    "v_cvt_pk_bf16_f32 v10, v14, v15\n\t" \
    "v_cvt_pk_bf16_f32 v11, v16, v17\n\t" \
    "v_cvt_pk_bf16_f32 v12, v18, v19\n\t" \
    "v_cvt_pk_bf16_f32 v13, v20, v21\n\t" \
    "s_mov_b64 exec, s[50:51]\n\t" \
    "ds_write_b128 v160, v[6:9] offset:0\n\t" \
    "ds_write_b128 v160, v[10:13] offset:640\n\t" \
    "s_mov_b64 exec, -1\n\t" \
    "v_cvt_pk_bf16_f32 v22, v22, v23\n\t" \
    "v_cvt_pk_bf16_f32 v23, v24, v25\n\t" \
    "v_cvt_pk_bf16_f32 v24, v26, v27\n\t" \
    "v_cvt_pk_bf16_f32 v25, v28, v29\n\t" \
    "v_cvt_pk_bf16_f32 v26, v30, v31\n\t" \
    "v_cvt_pk_bf16_f32 v27, v32, v33\n\t" \
    "v_cvt_pk_bf16_f32 v28, v34, v35\n\t" \
    "v_cvt_pk_bf16_f32 v29, v36, v37\n\t" \
    "s_mov_b64 exec, s[50:51]\n\t" \
    "ds_write_b128 v160, v[22:25] offset:1280\n\t" \
    "ds_write_b128 v160, v[26:29] offset:1920\n\t" \
    "s_mov_b64 exec, -1\n\t" \
    "v_mul_f32 v40, s49, v40\n\t" \
    "v_mul_f32 v41, s49, v41\n\t" \
    "v_mul_f32 v42, s49, v42\n\t" \
    "v_mul_f32 v43, s49, v43\n\t" \
    "v_mul_f32 v44, s49, v44\n\t" \
    "v_mul_f32 v45, s49, v45\n\t" \
    "v_mul_f32 v46, s49, v46\n\t" \
    "v_mul_f32 v47, s49, v47\n\t" \
    "v_mul_f32 v48, s49, v48\n\t" \
    "v_mul_f32 v49, s49, v49\n\t" \
    "v_mul_f32 v50, s49, v50\n\t" \
    "v_mul_f32 v51, s49, v51\n\t" \
    "v_mul_f32 v52, s49, v52\n\t" \
    "v_mul_f32 v53, s49, v53\n\t" \
    "v_mul_f32 v54, s49, v54\n\t" \
    "v_mul_f32 v55, s49, v55\n\t" \
    "s_waitcnt lgkmcnt(0)\n\t" \
    "s_barrier\n\t" \
    "v_cvt_pk_bf16_f32 v136, v40, v41\n\t" \
    "v_cvt_pk_bf16_f32 v137, v42, v43\n\t" \
    "v_cvt_pk_bf16_f32 v138, v44, v45\n\t" \
    "v_cvt_pk_bf16_f32 v139, v46, v47\n\t" \
    "v_cvt_pk_bf16_f32 v140, v48, v49\n\t" \
    "v_cvt_pk_bf16_f32 v141, v50, v51\n\t" \
    "v_cvt_pk_bf16_f32 v142, v52, v53\n\t" \
    "v_cvt_pk_bf16_f32 v143, v54, v55\n\t" \
    "v_mov_b32 v8, v56\n\t" \
    "v_mov_b32 v9, v57\n\t" \
    "v_mov_b32 v10, v58\n\t" \
    "v_mov_b32 v11, v59\n\t" \
    "v_mov_b32 v18, v90\n\t" \
    "v_mov_b32 v19, v91\n\t" \
    "v_permlane16_swap_b32 v136, v138\n\t" \
    "v_permlane16_swap_b32 v137, v139\n\t" \
    "v_permlane16_swap_b32 v140, v142\n\t" \
    "v_permlane16_swap_b32 v141, v143\n\t" \
    : "={v[104:119]}"(o[0]), "={v[120:135]}"(o[1]), "+{v[4:5]}"(xa), "+{v[0:3]}"(wa), "+{v[136:139]}"(qm[0]), "+{v[140:143]}"(qm[1]), "={v[144:145]}"(qr[0]), "={v[146:147]}"(qr[1]), "={v[8:11]}"(km0), "={v[18:19]}"(kr0) \
    : "{v158}"(cr), "{v159}"(akw), "{v160}"(avw), "{s49}"(sc), "{s[50:51]}"(vmask), "{s[52:53]}"(lomask), "{s58}"(wave), "{v[148:149]}"(ost), "{v150}"(aK0), "{v151}"(aV0), "{v152}"(ones), "{v156}"(vP), "{s[36:39]}"(osrd), "{s59}"(hw), "{s[68:71]}"(wsrd) \
    : "v6", "v7", "v12", "v13", "v14", "v15", "v16", "v17", "v20", "v21", "v22", "v23", "v24", "v25", "v26", "v27", "v28", "v29", "v30", "v31", "v32", "v33", "v34", "v35", "v36", "v37", "v38", "v39", "v40", "v41", "v42", "v43", "v44", "v45", "v46", "v47", "v48", "v49", "v50", "v51", "v52", "v53", "v54", "v55", "v56", "v57", "v58", "v59", "v60", "v61", "v62", "v63", "v64", "v65", "v66", "v67", "v68", "v69", "v70", "v71", "v72", "v73", "v74", "v75", "v76", "v77", "v78", "v79", "v80", "v81", "v82", "v83", "v84", "v85", "v86", "v87", "v88", "v89", "v90", "v91", "v92", "v93", "v94", "v95", "v96", "v97", "v98", "v99", "v100", "v101", "v102", "v103", "s46", "s47", "s48", "scc", "memory")

#define ATTN_CORE(o, qm, qr, km0, kr0, bad, probe, vP, osrd, aK0, aV0, ones, vmask, lomask, hw, ost, wsrd) \
  asm volatile( \
    "s_nop 1\n\t" \
    "v_mfma_f32_32x32x16_bf16 v[36:51], v[8:11], v[136:139], 0\n\t" \
    "v_mfma_f32_32x32x16_bf16 v[52:67], v[8:11], v[140:143], 0\n\t" \
    "v_mfma_f32_32x32x8_bf16 v[36:51], v[18:19], v[144:145], v[36:51]\n\t" \
    "v_mfma_f32_32x32x8_bf16 v[52:67], v[18:19], v[146:147], v[52:67]\n\t" \
    "s_mov_b32 s54, 0\n\t" \
    "s_mov_b32 s55, 40960\n\t" \
    "v_mov_b32 v0, v150\n\t" \
    "v_mov_b32 v1, v151\n\t" \
    "v_mov_b32 v2, 0xdddddddd\n\t" \
    "v_mov_b32 v20, v152\n\t" \
    "v_mov_b32 v21, v152\n\t" \
    "v_mov_b32 v22, v152\n\t" \
    "v_mov_b32 v23, v152\n\t" \
    "v_mov_b32 v24, v152\n\t" \
    "v_mov_b32 v25, v152\n\t" \
    "v_mov_b32 v26, v152\n\t" \
    "v_mov_b32 v27, v152\n\t" \
    "v_mov_b32 v28, v152\n\t" \
    "v_mov_b32 v29, v152\n\t" \
    "v_mov_b32 v30, v152\n\t" \
    "v_mov_b32 v31, v152\n\t" \
    "v_mov_b32 v32, v152\n\t" \
    "v_mov_b32 v33, v152\n\t" \
    "v_mov_b32 v34, v152\n\t" \
    "v_mov_b32 v35, v152\n\t" \
    "v_mov_b32 v12, 0x3f80\n\t" \
    "v_mov_b32 v13, 0\n\t" \
    "v_mov_b32 v14, 0x3f80\n\t" \
    "v_mov_b32 v15, 0\n\t" \
    "ds_read_b128 v[4:7], v0 offset:0\n\t" \
    "s_mov_b64 exec, s[52:53]\n\t" \
    "ds_read_b128 v[12:15], v0 offset:2048\n\t" \
    "s_mov_b64 exec, s[50:51]\n\t" \
    "ds_read_b128 v[20:23], v1 offset:0\n\t" \
    "ds_read_b128 v[24:27], v1 offset:640\n\t" \
    "s_mov_b64 exec, -1\n\t" \
    "ds_read_b128 v[8:11], v0 offset:1024\n\t" \
    "v_mov_b32 v16, 0x3f80\n\t" \
    "v_mov_b32 v17, 0\n\t" \
    "v_mov_b32 v18, 0x3f80\n\t" \
    "v_mov_b32 v19, 0\n\t" \
    "v_mov_b32 v92, 0\n\t" \
    "v_mov_b32 v93, 0\n\t" \
    "v_mov_b32 v94, 0\n\t" \
    "v_mov_b32 v95, 0\n\t" \
    "v_mov_b32 v96, 0\n\t" \
    "v_mov_b32 v97, 0\n\t" \
    "v_mov_b32 v98, 0\n\t" \
    "v_mov_b32 v99, 0\n\t" \
    "v_max3_f32 v68, v36, v37, v38\n\t" \
    "v_max3_f32 v70, v52, v53, v54\n\t" \
    "v_max3_f32 v68, v68, v39, v40\n\t" \
    "v_max3_f32 v70, v70, v55, v56\n\t" \
    "v_max3_f32 v68, v68, v41, v42\n\t" \
    "v_max3_f32 v70, v70, v57, v58\n\t" \
    "v_max3_f32 v68, v68, v43, v44\n\t" \
    "v_max3_f32 v70, v70, v59, v60\n\t" \
    "v_max3_f32 v68, v68, v45, v46\n\t" \
    "v_max3_f32 v70, v70, v61, v62\n\t" \
    "v_max3_f32 v68, v68, v47, v48\n\t" \
    "v_max3_f32 v70, v70, v63, v64\n\t" \
    "v_max3_f32 v68, v68, v49, v50\n\t" \
    "v_max3_f32 v70, v70, v65, v66\n\t" \
    "v_max_f32 v68, v68, v51\n\t" \
    "v_max_f32 v70, v70, v67\n\t" \
    "v_mov_b32 v69, v68\n\t" \
    "v_mov_b32 v71, v70\n\t" \
    "s_nop 0\n\t" \
    "v_permlane32_swap_b32 v68, v69\n\t" \
    "v_permlane32_swap_b32 v70, v71\n\t" \
    "s_nop 0\n\t" \
    "v_max_f32 v68, v68, v69\n\t" \
    "v_max_f32 v70, v70, v71\n\t" \
    "s_not_b64 exec, s[52:53]\n\t" \
    "v_cvt_pk_bf16_f32 v144, -v68, 0\n\t" \
    "v_cvt_pk_bf16_f32 v146, -v70, 0\n\t" \
    "s_mov_b64 exec, -1\n\t" \
    "s_waitcnt lgkmcnt(3)\n\t" \
    "v_mfma_f32_32x32x16_bf16 v[36:51], v[4:7], v[136:139], 0\n\t" \
    "v_mfma_f32_32x32x8_bf16 v[36:51], v[12:13], v[144:145], v[36:51]\n\t" \
    "s_waitcnt lgkmcnt(0)\n\t" \
    "s_nop 9\n\t" \
    "s_cmp_ge_u32 s58, 4\n\t" \
    "s_cbranch_scc0 NPR_%=\n\t" \
    "s_setprio 1\n\t" \
    "NPR_%=:\n\t" \
    "LOOP_%=:\n\t" \
    "v_mfma_f32_32x32x16_bf16 v[52:67], v[4:7], v[140:143], 0\n\t" \
    "v_exp_f32 v36, v36\n\t" \
    "v_exp_f32 v37, v37\n\t" \
    "v_exp_f32 v38, v38\n\t" \
    "v_exp_f32 v39, v39\n\t" \
    "v_mfma_f32_32x32x8_bf16 v[52:67], v[12:13], v[146:147], v[52:67]\n\t" \
    "v_exp_f32 v40, v40\n\t" \
    "v_exp_f32 v41, v41\n\t" \
    "v_exp_f32 v42, v42\n\t" \
    "v_exp_f32 v43, v43\n\t" \
    "v_smfmac_f32_32x32x32_bf16 v[120:135], v[32:35], v[92:99], v2\n\t" \
    "s_waitcnt lgkmcnt(0)\n\t" \
    "s_mov_b64 exec, s[50:51]\n\t" \
    "ds_read_b128 v[28:31], v1 offset:1280\n\t" \
    "s_mov_b64 exec, -1\n\t" \
    "v_exp_f32 v44, v44\n\t" \
    "v_exp_f32 v45, v45\n\t" \
    "v_exp_f32 v46, v46\n\t" \
    "v_exp_f32 v47, v47\n\t" \
    "v_smfmac_f32_32x32x32_bf16 v[104:119], v[20:23], v[36:43], v2\n\t" \
    "s_mov_b64 exec, s[50:51]\n\t" \
    "ds_read_b128 v[32:35], v1 offset:1920\n\t" \
    "s_mov_b64 exec, -1\n\t" \
    "v_exp_f32 v48, v48\n\t" \
    "v_exp_f32 v49, v49\n\t" \
    "v_exp_f32 v50, v50\n\t" \
    "v_exp_f32 v51, v51\n\t" \
    "v_mfma_f32_32x32x16_bf16 v[68:83], v[8:11], v[136:139], 0\n\t" \
    "v_exp_f32 v52, v52\n\t" \
    "v_exp_f32 v53, v53\n\t" \
    "v_exp_f32 v54, v54\n\t" \
    "v_exp_f32 v55, v55\n\t" \
    "v_mfma_f32_32x32x8_bf16 v[68:83], v[14:15], v[144:145], v[68:83]\n\t" \
    "v_exp_f32 v56, v56\n\t" \
    "v_exp_f32 v57, v57\n\t" \
    "v_exp_f32 v58, v58\n\t" \
    "v_exp_f32 v59, v59\n\t" \
    "v_smfmac_f32_32x32x32_bf16 v[104:119], v[24:27], v[44:51], v2\n\t" \
    "ds_read_b128 v[4:7], v0 offset:5120\n\t" \
    "v_exp_f32 v60, v60\n\t" \
    "v_exp_f32 v61, v61\n\t" \
    "v_exp_f32 v62, v62\n\t" \
    "v_exp_f32 v63, v63\n\t" \
    "v_smfmac_f32_32x32x32_bf16 v[120:135], v[20:23], v[52:59], v2\n\t" \
    "s_mov_b64 exec, s[52:53]\n\t" \
    "ds_read_b128 v[16:19], v0 offset:7168\n\t" \
    "s_mov_b64 exec, -1\n\t" \
    "v_exp_f32 v64, v64\n\t" \
    "v_exp_f32 v65, v65\n\t" \
    "v_exp_f32 v66, v66\n\t" \
    "v_exp_f32 v67, v67\n\t" \
    "v_mfma_f32_32x32x16_bf16 v[84:99], v[8:11], v[140:143], 0\n\t" \
    "v_exp_f32 v68, v68\n\t" \
    "v_exp_f32 v69, v69\n\t" \
    "v_exp_f32 v70, v70\n\t" \
    "v_exp_f32 v71, v71\n\t" \
    "v_mfma_f32_32x32x8_bf16 v[84:99], v[14:15], v[146:147], v[84:99]\n\t" \
    "v_exp_f32 v72, v72\n\t" \
    "v_exp_f32 v73, v73\n\t" \
    "v_exp_f32 v74, v74\n\t" \
    "v_exp_f32 v75, v75\n\t" \
    "v_smfmac_f32_32x32x32_bf16 v[120:135], v[24:27], v[60:67], v2\n\t" \
    "s_waitcnt lgkmcnt(0)\n\t" \
    "s_mov_b64 exec, s[50:51]\n\t" \
    "ds_read_b128 v[20:23], v1 offset:5120\n\t" \
    "s_mov_b64 exec, -1\n\t" \
    "v_exp_f32 v76, v76\n\t" \
    "v_exp_f32 v77, v77\n\t" \
    "v_exp_f32 v78, v78\n\t" \
    "v_exp_f32 v79, v79\n\t" \
    "v_smfmac_f32_32x32x32_bf16 v[104:119], v[28:31], v[68:75], v2\n\t" \
    "s_mov_b64 exec, s[50:51]\n\t" \
    "ds_read_b128 v[24:27], v1 offset:5760\n\t" \
    "s_mov_b64 exec, -1\n\t" \
    "v_exp_f32 v80, v80\n\t" \
    "v_exp_f32 v81, v81\n\t" \
    "v_exp_f32 v82, v82\n\t" \
    "v_exp_f32 v83, v83\n\t" \
    "v_mfma_f32_32x32x16_bf16 v[36:51], v[4:7], v[136:139], 0\n\t" \
    "v_exp_f32 v84, v84\n\t" \
    "v_exp_f32 v85, v85\n\t" \
    "v_exp_f32 v86, v86\n\t" \
    "v_exp_f32 v87, v87\n\t" \
    "v_mfma_f32_32x32x8_bf16 v[36:51], v[16:17], v[144:145], v[36:51]\n\t" \
    "v_exp_f32 v88, v88\n\t" \
    "v_exp_f32 v89, v89\n\t" \
    "v_exp_f32 v90, v90\n\t" \
    "v_exp_f32 v91, v91\n\t" \
    "v_smfmac_f32_32x32x32_bf16 v[104:119], v[32:35], v[76:83], v2\n\t" \
    "ds_read_b128 v[8:11], v0 offset:6144\n\t" \
    "v_exp_f32 v92, v92\n\t" \
    "v_exp_f32 v93, v93\n\t" \
    "v_exp_f32 v94, v94\n\t" \
    "v_exp_f32 v95, v95\n\t" \
    "v_smfmac_f32_32x32x32_bf16 v[120:135], v[28:31], v[84:91], v2\n\t" \
    "v_exp_f32 v96, v96\n\t" \
    "v_exp_f32 v97, v97\n\t" \
    "v_exp_f32 v98, v98\n\t" \
    "v_exp_f32 v99, v99\n\t" \
    "v_mfma_f32_32x32x16_bf16 v[52:67], v[4:7], v[140:143], 0\n\t" \
    "v_exp_f32 v36, v36\n\t" \
    "v_exp_f32 v37, v37\n\t" \
    "v_exp_f32 v38, v38\n\t" \
    "v_exp_f32 v39, v39\n\t" \
    "v_mfma_f32_32x32x8_bf16 v[52:67], v[16:17], v[146:147], v[52:67]\n\t" \
    "v_exp_f32 v40, v40\n\t" \
    "v_exp_f32 v41, v41\n\t" \
    "v_exp_f32 v42, v42\n\t" \
    "v_exp_f32 v43, v43\n\t" \
    "v_smfmac_f32_32x32x32_bf16 v[120:135], v[32:35], v[92:99], v2\n\t" \
    "s_waitcnt lgkmcnt(0)\n\t" \
    "s_mov_b64 exec, s[50:51]\n\t" \
    "ds_read_b128 v[28:31], v1 offset:6400\n\t" \
    "s_mov_b64 exec, -1\n\t" \
    "v_exp_f32 v44, v44\n\t" \
    "v_exp_f32 v45, v45\n\t" \
    "v_exp_f32 v46, v46\n\t" \
    "v_exp_f32 v47, v47\n\t" \
    "v_smfmac_f32_32x32x32_bf16 v[104:119], v[20:23], v[36:43], v2\n\t" \
    "s_mov_b64 exec, s[50:51]\n\t" \
    "ds_read_b128 v[32:35], v1 offset:7040\n\t" \
    "s_mov_b64 exec, -1\n\t" \
    "v_exp_f32 v48, v48\n\t" \
    "v_exp_f32 v49, v49\n\t" \
    "v_exp_f32 v50, v50\n\t" \
    "v_exp_f32 v51, v51\n\t" \
    "v_mfma_f32_32x32x16_bf16 v[68:83], v[8:11], v[136:139], 0\n\t" \
    "v_exp_f32 v52, v52\n\t" \
    "v_exp_f32 v53, v53\n\t" \
    "v_exp_f32 v54, v54\n\t" \
    "v_exp_f32 v55, v55\n\t" \
    "v_mfma_f32_32x32x8_bf16 v[68:83], v[18:19], v[144:145], v[68:83]\n\t" \
    "v_exp_f32 v56, v56\n\t" \
    "v_exp_f32 v57, v57\n\t" \
    "v_exp_f32 v58, v58\n\t" \
    "v_exp_f32 v59, v59\n\t" \
    "v_smfmac_f32_32x32x32_bf16 v[104:119], v[24:27], v[44:51], v2\n\t" \
    "ds_read_b128 v[4:7], v0 offset:10240\n\t" \
    "v_exp_f32 v60, v60\n\t" \
    "v_exp_f32 v61, v61\n\t" \
    "v_exp_f32 v62, v62\n\t" \
    "v_exp_f32 v63, v63\n\t" \
    "v_smfmac_f32_32x32x32_bf16 v[120:135], v[20:23], v[52:59], v2\n\t" \
    "s_mov_b64 exec, s[52:53]\n\t" \
    "ds_read_b128 v[12:15], v0 offset:12288\n\t" \
    "s_mov_b64 exec, -1\n\t" \
    "v_exp_f32 v64, v64\n\t" \
    "v_exp_f32 v65, v65\n\t" \
    "v_exp_f32 v66, v66\n\t" \
    "v_exp_f32 v67, v67\n\t" \
    "v_mfma_f32_32x32x16_bf16 v[84:99], v[8:11], v[140:143], 0\n\t" \
    "v_exp_f32 v68, v68\n\t" \
    "v_exp_f32 v69, v69\n\t" \
    "v_exp_f32 v70, v70\n\t" \
    "v_exp_f32 v71, v71\n\t" \
    "v_mfma_f32_32x32x8_bf16 v[84:99], v[18:19], v[146:147], v[84:99]\n\t" \
    "v_exp_f32 v72, v72\n\t" \
    "v_exp_f32 v73, v73\n\t" \
    "v_exp_f32 v74, v74\n\t" \
    "v_exp_f32 v75, v75\n\t" \
    "v_smfmac_f32_32x32x32_bf16 v[120:135], v[24:27], v[60:67], v2\n\t" \
    "s_waitcnt lgkmcnt(0)\n\t" \
    "s_mov_b64 exec, s[50:51]\n\t" \
    "ds_read_b128 v[20:23], v1 offset:10240\n\t" \
    "s_mov_b64 exec, -1\n\t" \
    "v_exp_f32 v76, v76\n\t" \
    "v_exp_f32 v77, v77\n\t" \
    "v_exp_f32 v78, v78\n\t" \
    "v_exp_f32 v79, v79\n\t" \
    "v_smfmac_f32_32x32x32_bf16 v[104:119], v[28:31], v[68:75], v2\n\t" \
    "s_mov_b64 exec, s[50:51]\n\t" \
    "ds_read_b128 v[24:27], v1 offset:10880\n\t" \
    "s_mov_b64 exec, -1\n\t" \
    "v_exp_f32 v80, v80\n\t" \
    "v_exp_f32 v81, v81\n\t" \
    "v_exp_f32 v82, v82\n\t" \
    "v_exp_f32 v83, v83\n\t" \
    "v_mfma_f32_32x32x16_bf16 v[36:51], v[4:7], v[136:139], 0\n\t" \
    "v_exp_f32 v84, v84\n\t" \
    "v_exp_f32 v85, v85\n\t" \
    "v_exp_f32 v86, v86\n\t" \
    "v_exp_f32 v87, v87\n\t" \
    "v_mfma_f32_32x32x8_bf16 v[36:51], v[12:13], v[144:145], v[36:51]\n\t" \
    "v_exp_f32 v88, v88\n\t" \
    "v_exp_f32 v89, v89\n\t" \
    "v_exp_f32 v90, v90\n\t" \
    "v_exp_f32 v91, v91\n\t" \
    "v_smfmac_f32_32x32x32_bf16 v[104:119], v[32:35], v[76:83], v2\n\t" \
    "ds_read_b128 v[8:11], v0 offset:11264\n\t" \
    "v_exp_f32 v92, v92\n\t" \
    "v_exp_f32 v93, v93\n\t" \
    "v_exp_f32 v94, v94\n\t" \
    "v_exp_f32 v95, v95\n\t" \
    "v_smfmac_f32_32x32x32_bf16 v[120:135], v[28:31], v[84:91], v2\n\t" \
    "v_exp_f32 v96, v96\n\t" \
    "v_exp_f32 v97, v97\n\t" \
    "v_exp_f32 v98, v98\n\t" \
    "v_exp_f32 v99, v99\n\t" \
    "v_mfma_f32_32x32x16_bf16 v[52:67], v[4:7], v[140:143], 0\n\t" \
    "v_exp_f32 v36, v36\n\t" \
    "v_exp_f32 v37, v37\n\t" \
    "v_exp_f32 v38, v38\n\t" \
    "v_exp_f32 v39, v39\n\t" \
    "v_mfma_f32_32x32x8_bf16 v[52:67], v[12:13], v[146:147], v[52:67]\n\t" \
    "v_exp_f32 v40, v40\n\t" \
    "v_exp_f32 v41, v41\n\t" \
    "v_exp_f32 v42, v42\n\t" \
    "v_exp_f32 v43, v43\n\t" \
    "v_smfmac_f32_32x32x32_bf16 v[120:135], v[32:35], v[92:99], v2\n\t" \
    "s_waitcnt lgkmcnt(0)\n\t" \
    "s_mov_b64 exec, s[50:51]\n\t" \
    "ds_read_b128 v[28:31], v1 offset:11520\n\t" \
    "s_mov_b64 exec, -1\n\t" \
    "v_exp_f32 v44, v44\n\t" \
    "v_exp_f32 v45, v45\n\t" \
    "v_exp_f32 v46, v46\n\t" \
    "v_exp_f32 v47, v47\n\t" \
    "v_smfmac_f32_32x32x32_bf16 v[104:119], v[20:23], v[36:43], v2\n\t" \
    "s_mov_b64 exec, s[50:51]\n\t" \
    "ds_read_b128 v[32:35], v1 offset:12160\n\t" \
    "s_mov_b64 exec, -1\n\t" \
    "v_exp_f32 v48, v48\n\t" \
    "v_exp_f32 v49, v49\n\t" \
    "v_exp_f32 v50, v50\n\t" \
    "v_exp_f32 v51, v51\n\t" \
    "v_mfma_f32_32x32x16_bf16 v[68:83], v[8:11], v[136:139], 0\n\t" \
    "v_exp_f32 v52, v52\n\t" \
    "v_exp_f32 v53, v53\n\t" \
    "v_exp_f32 v54, v54\n\t" \
    "v_exp_f32 v55, v55\n\t" \
    "v_mfma_f32_32x32x8_bf16 v[68:83], v[14:15], v[144:145], v[68:83]\n\t" \
    "v_exp_f32 v56, v56\n\t" \
    "v_exp_f32 v57, v57\n\t" \
    "v_exp_f32 v58, v58\n\t" \
    "v_exp_f32 v59, v59\n\t" \
    "v_smfmac_f32_32x32x32_bf16 v[104:119], v[24:27], v[44:51], v2\n\t" \
    "ds_read_b128 v[4:7], v0 offset:15360\n\t" \
    "v_exp_f32 v60, v60\n\t" \
    "v_exp_f32 v61, v61\n\t" \
    "v_exp_f32 v62, v62\n\t" \
    "v_exp_f32 v63, v63\n\t" \
    "v_smfmac_f32_32x32x32_bf16 v[120:135], v[20:23], v[52:59], v2\n\t" \
    "s_mov_b64 exec, s[52:53]\n\t" \
    "ds_read_b128 v[16:19], v0 offset:17408\n\t" \
    "s_mov_b64 exec, -1\n\t" \
    "v_exp_f32 v64, v64\n\t" \
    "v_exp_f32 v65, v65\n\t" \
    "v_exp_f32 v66, v66\n\t" \
    "v_exp_f32 v67, v67\n\t" \
    "v_mfma_f32_32x32x16_bf16 v[84:99], v[8:11], v[140:143], 0\n\t" \
    "v_exp_f32 v68, v68\n\t" \
    "v_exp_f32 v69, v69\n\t" \
    "v_exp_f32 v70, v70\n\t" \
    "v_exp_f32 v71, v71\n\t" \
    "v_mfma_f32_32x32x8_bf16 v[84:99], v[14:15], v[146:147], v[84:99]\n\t" \
    "v_exp_f32 v72, v72\n\t" \
    "v_exp_f32 v73, v73\n\t" \
    "v_exp_f32 v74, v74\n\t" \
    "v_exp_f32 v75, v75\n\t" \
    "v_smfmac_f32_32x32x32_bf16 v[120:135], v[24:27], v[60:67], v2\n\t" \
    "s_waitcnt lgkmcnt(0)\n\t" \
    "s_mov_b64 exec, s[50:51]\n\t" \
    "ds_read_b128 v[20:23], v1 offset:15360\n\t" \
    "s_mov_b64 exec, -1\n\t" \
    "v_exp_f32 v76, v76\n\t" \
    "v_exp_f32 v77, v77\n\t" \
    "v_exp_f32 v78, v78\n\t" \
    "v_exp_f32 v79, v79\n\t" \
    "v_smfmac_f32_32x32x32_bf16 v[104:119], v[28:31], v[68:75], v2\n\t" \
    "s_mov_b64 exec, s[50:51]\n\t" \
    "ds_read_b128 v[24:27], v1 offset:16000\n\t" \
    "s_mov_b64 exec, -1\n\t" \
    "v_exp_f32 v80, v80\n\t" \
    "v_exp_f32 v81, v81\n\t" \
    "v_exp_f32 v82, v82\n\t" \
    "v_exp_f32 v83, v83\n\t" \
    "v_mfma_f32_32x32x16_bf16 v[36:51], v[4:7], v[136:139], 0\n\t" \
    "v_exp_f32 v84, v84\n\t" \
    "v_exp_f32 v85, v85\n\t" \
    "v_exp_f32 v86, v86\n\t" \
    "v_exp_f32 v87, v87\n\t" \
    "v_mfma_f32_32x32x8_bf16 v[36:51], v[16:17], v[144:145], v[36:51]\n\t" \
    "v_exp_f32 v88, v88\n\t" \
    "v_exp_f32 v89, v89\n\t" \
    "v_exp_f32 v90, v90\n\t" \
    "v_exp_f32 v91, v91\n\t" \
    "v_smfmac_f32_32x32x32_bf16 v[104:119], v[32:35], v[76:83], v2\n\t" \
    "ds_read_b128 v[8:11], v0 offset:16384\n\t" \
    "v_exp_f32 v92, v92\n\t" \
    "v_exp_f32 v93, v93\n\t" \
    "v_exp_f32 v94, v94\n\t" \
    "v_exp_f32 v95, v95\n\t" \
    "v_smfmac_f32_32x32x32_bf16 v[120:135], v[28:31], v[84:91], v2\n\t" \
    "v_exp_f32 v96, v96\n\t" \
    "v_exp_f32 v97, v97\n\t" \
    "v_exp_f32 v98, v98\n\t" \
    "v_exp_f32 v99, v99\n\t" \
    "v_mfma_f32_32x32x16_bf16 v[52:67], v[4:7], v[140:143], 0\n\t" \
    "v_exp_f32 v36, v36\n\t" \
    "v_exp_f32 v37, v37\n\t" \
    "v_exp_f32 v38, v38\n\t" \
    "v_exp_f32 v39, v39\n\t" \
    "v_mfma_f32_32x32x8_bf16 v[52:67], v[16:17], v[146:147], v[52:67]\n\t" \
    "v_exp_f32 v40, v40\n\t" \
    "v_exp_f32 v41, v41\n\t" \
    "v_exp_f32 v42, v42\n\t" \
    "v_exp_f32 v43, v43\n\t" \
    "v_smfmac_f32_32x32x32_bf16 v[120:135], v[32:35], v[92:99], v2\n\t" \
    "s_waitcnt lgkmcnt(0)\n\t" \
    "s_mov_b64 exec, s[50:51]\n\t" \
    "ds_read_b128 v[28:31], v1 offset:16640\n\t" \
    "s_mov_b64 exec, -1\n\t" \
    "v_exp_f32 v44, v44\n\t" \
    "v_exp_f32 v45, v45\n\t" \
    "v_exp_f32 v46, v46\n\t" \
    "v_exp_f32 v47, v47\n\t" \
    "v_smfmac_f32_32x32x32_bf16 v[104:119], v[20:23], v[36:43], v2\n\t" \
    "s_mov_b64 exec, s[50:51]\n\t" \
    "ds_read_b128 v[32:35], v1 offset:17280\n\t" \
    "s_mov_b64 exec, -1\n\t" \
    "v_exp_f32 v48, v48\n\t" \
    "v_exp_f32 v49, v49\n\t" \
    "v_exp_f32 v50, v50\n\t" \
    "v_exp_f32 v51, v51\n\t" \
    "v_mfma_f32_32x32x16_bf16 v[68:83], v[8:11], v[136:139], 0\n\t" \
    "v_exp_f32 v52, v52\n\t" \
    "v_exp_f32 v53, v53\n\t" \
    "v_exp_f32 v54, v54\n\t" \
    "v_exp_f32 v55, v55\n\t" \
    "v_mfma_f32_32x32x8_bf16 v[68:83], v[18:19], v[144:145], v[68:83]\n\t" \
    "v_exp_f32 v56, v56\n\t" \
    "v_exp_f32 v57, v57\n\t" \
    "v_exp_f32 v58, v58\n\t" \
    "v_exp_f32 v59, v59\n\t" \
    "v_smfmac_f32_32x32x32_bf16 v[104:119], v[24:27], v[44:51], v2\n\t" \
    "s_waitcnt lgkmcnt(0)\n\t" \
    "s_barrier\n\t" \
    "ds_read_b128 v[4:7], v0 offset:40960\n\t" \
    "v_exp_f32 v60, v60\n\t" \
    "v_exp_f32 v61, v61\n\t" \
    "v_exp_f32 v62, v62\n\t" \
    "v_exp_f32 v63, v63\n\t" \
    "v_smfmac_f32_32x32x32_bf16 v[120:135], v[20:23], v[52:59], v2\n\t" \
    "s_mov_b64 exec, s[52:53]\n\t" \
    "ds_read_b128 v[12:15], v0 offset:43008\n\t" \
    "s_mov_b64 exec, -1\n\t" \
    "v_exp_f32 v64, v64\n\t" \
    "v_exp_f32 v65, v65\n\t" \
    "v_exp_f32 v66, v66\n\t" \
    "v_exp_f32 v67, v67\n\t" \
    "v_mfma_f32_32x32x16_bf16 v[84:99], v[8:11], v[140:143], 0\n\t" \
    "v_exp_f32 v68, v68\n\t" \
    "v_exp_f32 v69, v69\n\t" \
    "v_exp_f32 v70, v70\n\t" \
    "v_exp_f32 v71, v71\n\t" \
    "v_mfma_f32_32x32x8_bf16 v[84:99], v[18:19], v[146:147], v[84:99]\n\t" \
    "v_exp_f32 v72, v72\n\t" \
    "v_exp_f32 v73, v73\n\t" \
    "v_exp_f32 v74, v74\n\t" \
    "v_exp_f32 v75, v75\n\t" \
    "v_smfmac_f32_32x32x32_bf16 v[120:135], v[24:27], v[60:67], v2\n\t" \
    "s_waitcnt lgkmcnt(0)\n\t" \
    "s_mov_b64 exec, s[50:51]\n\t" \
    "ds_read_b128 v[20:23], v1 offset:40960\n\t" \
    "s_mov_b64 exec, -1\n\t" \
    "v_exp_f32 v76, v76\n\t" \
    "v_exp_f32 v77, v77\n\t" \
    "v_exp_f32 v78, v78\n\t" \
    "v_exp_f32 v79, v79\n\t" \
    "v_smfmac_f32_32x32x32_bf16 v[104:119], v[28:31], v[68:75], v2\n\t" \
    "s_mov_b64 exec, s[50:51]\n\t" \
    "ds_read_b128 v[24:27], v1 offset:41600\n\t" \
    "s_mov_b64 exec, -1\n\t" \
    "v_exp_f32 v80, v80\n\t" \
    "v_exp_f32 v81, v81\n\t" \
    "v_exp_f32 v82, v82\n\t" \
    "v_exp_f32 v83, v83\n\t" \
    "v_mfma_f32_32x32x16_bf16 v[36:51], v[4:7], v[136:139], 0\n\t" \
    "v_exp_f32 v84, v84\n\t" \
    "v_exp_f32 v85, v85\n\t" \
    "v_exp_f32 v86, v86\n\t" \
    "v_exp_f32 v87, v87\n\t" \
    "v_mfma_f32_32x32x8_bf16 v[36:51], v[12:13], v[144:145], v[36:51]\n\t" \
    "v_exp_f32 v88, v88\n\t" \
    "v_exp_f32 v89, v89\n\t" \
    "v_exp_f32 v90, v90\n\t" \
    "v_exp_f32 v91, v91\n\t" \
    "v_smfmac_f32_32x32x32_bf16 v[104:119], v[32:35], v[76:83], v2\n\t" \
    "ds_read_b128 v[8:11], v0 offset:41984\n\t" \
    "v_exp_f32 v92, v92\n\t" \
    "v_exp_f32 v93, v93\n\t" \
    "v_exp_f32 v94, v94\n\t" \
    "v_exp_f32 v95, v95\n\t" \
    "v_smfmac_f32_32x32x32_bf16 v[120:135], v[28:31], v[84:91], v2\n\t" \
    "s_add_u32 s54, s54, 1\n\t" \
    "v_exp_f32 v96, v96\n\t" \
    "v_exp_f32 v97, v97\n\t" \
    "v_exp_f32 v98, v98\n\t" \
    "v_exp_f32 v99, v99\n\t" \
    "v_mfma_f32_32x32x16_bf16 v[52:67], v[4:7], v[140:143], 0\n\t" \
    "v_exp_f32 v36, v36\n\t" \
    "v_exp_f32 v37, v37\n\t" \
    "v_exp_f32 v38, v38\n\t" \
    "v_exp_f32 v39, v39\n\t" \
    "v_mfma_f32_32x32x8_bf16 v[52:67], v[12:13], v[146:147], v[52:67]\n\t" \
    "v_exp_f32 v40, v40\n\t" \
    "v_exp_f32 v41, v41\n\t" \
    "v_exp_f32 v42, v42\n\t" \
    "v_exp_f32 v43, v43\n\t" \
    "v_smfmac_f32_32x32x32_bf16 v[120:135], v[32:35], v[92:99], v2\n\t" \
    "s_waitcnt lgkmcnt(0)\n\t" \
    "s_mov_b64 exec, s[50:51]\n\t" \
    "ds_read_b128 v[28:31], v1 offset:42240\n\t" \
    "s_mov_b64 exec, -1\n\t" \
    "v_exp_f32 v44, v44\n\t" \
    "v_exp_f32 v45, v45\n\t" \
    "v_exp_f32 v46, v46\n\t" \
    "v_exp_f32 v47, v47\n\t" \
    "v_smfmac_f32_32x32x32_bf16 v[104:119], v[20:23], v[36:43], v2\n\t" \
    "s_mov_b64 exec, s[50:51]\n\t" \
    "ds_read_b128 v[32:35], v1 offset:42880\n\t" \
    "s_mov_b64 exec, -1\n\t" \
    "v_exp_f32 v48, v48\n\t" \
    "v_exp_f32 v49, v49\n\t" \
    "v_exp_f32 v50, v50\n\t" \
    "v_exp_f32 v51, v51\n\t" \
    "v_mfma_f32_32x32x16_bf16 v[68:83], v[8:11], v[136:139], 0\n\t" \
    "v_exp_f32 v52, v52\n\t" \
    "v_exp_f32 v53, v53\n\t" \
    "v_exp_f32 v54, v54\n\t" \
    "v_exp_f32 v55, v55\n\t" \
    "v_mfma_f32_32x32x8_bf16 v[68:83], v[14:15], v[144:145], v[68:83]\n\t" \
    "v_exp_f32 v56, v56\n\t" \
    "v_exp_f32 v57, v57\n\t" \
    "v_exp_f32 v58, v58\n\t" \
    "v_exp_f32 v59, v59\n\t" \
    "v_smfmac_f32_32x32x32_bf16 v[104:119], v[24:27], v[44:51], v2\n\t" \
    "ds_read_b128 v[4:7], v0 offset:46080\n\t" \
    "v_exp_f32 v60, v60\n\t" \
    "v_exp_f32 v61, v61\n\t" \
    "v_exp_f32 v62, v62\n\t" \
    "v_exp_f32 v63, v63\n\t" \
    "v_smfmac_f32_32x32x32_bf16 v[120:135], v[20:23], v[52:59], v2\n\t" \
    "s_mov_b64 exec, s[52:53]\n\t" \
    "ds_read_b128 v[16:19], v0 offset:48128\n\t" \
    "s_mov_b64 exec, -1\n\t" \
    "v_exp_f32 v64, v64\n\t" \
    "v_exp_f32 v65, v65\n\t" \
    "v_exp_f32 v66, v66\n\t" \
    "v_exp_f32 v67, v67\n\t" \
    "v_mfma_f32_32x32x16_bf16 v[84:99], v[8:11], v[140:143], 0\n\t" \
    "v_exp_f32 v68, v68\n\t" \
    "v_exp_f32 v69, v69\n\t" \
    "v_exp_f32 v70, v70\n\t" \
    "v_exp_f32 v71, v71\n\t" \
    "v_mfma_f32_32x32x8_bf16 v[84:99], v[14:15], v[146:147], v[84:99]\n\t" \
    "v_exp_f32 v72, v72\n\t" \
    "v_exp_f32 v73, v73\n\t" \
    "v_exp_f32 v74, v74\n\t" \
    "v_exp_f32 v75, v75\n\t" \
    "v_smfmac_f32_32x32x32_bf16 v[120:135], v[24:27], v[60:67], v2\n\t" \
    "s_waitcnt lgkmcnt(0)\n\t" \
    "s_mov_b64 exec, s[50:51]\n\t" \
    "ds_read_b128 v[20:23], v1 offset:46080\n\t" \
    "s_mov_b64 exec, -1\n\t" \
    "v_exp_f32 v76, v76\n\t" \
    "v_exp_f32 v77, v77\n\t" \
    "v_exp_f32 v78, v78\n\t" \
    "v_exp_f32 v79, v79\n\t" \
    "v_smfmac_f32_32x32x32_bf16 v[104:119], v[28:31], v[68:75], v2\n\t" \
    "s_mov_b64 exec, s[50:51]\n\t" \
    "ds_read_b128 v[24:27], v1 offset:46720\n\t" \
    "s_mov_b64 exec, -1\n\t" \
    "v_exp_f32 v80, v80\n\t" \
    "v_exp_f32 v81, v81\n\t" \
    "v_exp_f32 v82, v82\n\t" \
    "v_exp_f32 v83, v83\n\t" \
    "v_mfma_f32_32x32x16_bf16 v[36:51], v[4:7], v[136:139], 0\n\t" \
    "v_exp_f32 v84, v84\n\t" \
    "v_exp_f32 v85, v85\n\t" \
    "v_exp_f32 v86, v86\n\t" \
    "v_exp_f32 v87, v87\n\t" \
    "v_mfma_f32_32x32x8_bf16 v[36:51], v[16:17], v[144:145], v[36:51]\n\t" \
    "v_exp_f32 v88, v88\n\t" \
    "v_exp_f32 v89, v89\n\t" \
    "v_exp_f32 v90, v90\n\t" \
    "v_exp_f32 v91, v91\n\t" \
    "v_smfmac_f32_32x32x32_bf16 v[104:119], v[32:35], v[76:83], v2\n\t" \
    "ds_read_b128 v[8:11], v0 offset:47104\n\t" \
    "v_exp_f32 v92, v92\n\t" \
    "v_exp_f32 v93, v93\n\t" \
    "v_exp_f32 v94, v94\n\t" \
    "v_exp_f32 v95, v95\n\t" \
    "v_smfmac_f32_32x32x32_bf16 v[120:135], v[28:31], v[84:91], v2\n\t" \
    "v_exp_f32 v96, v96\n\t" \
    "v_exp_f32 v97, v97\n\t" \
    "v_exp_f32 v98, v98\n\t" \
    "v_exp_f32 v99, v99\n\t" \
    "v_mfma_f32_32x32x16_bf16 v[52:67], v[4:7], v[140:143], 0\n\t" \
    "v_exp_f32 v36, v36\n\t" \
    "v_exp_f32 v37, v37\n\t" \
    "v_exp_f32 v38, v38\n\t" \
    "v_exp_f32 v39, v39\n\t" \
    "v_mfma_f32_32x32x8_bf16 v[52:67], v[16:17], v[146:147], v[52:67]\n\t" \
    "v_exp_f32 v40, v40\n\t" \
    "v_exp_f32 v41, v41\n\t" \
    "v_exp_f32 v42, v42\n\t" \
    "v_exp_f32 v43, v43\n\t" \
    "v_smfmac_f32_32x32x32_bf16 v[120:135], v[32:35], v[92:99], v2\n\t" \
    "s_waitcnt lgkmcnt(0)\n\t" \
    "s_mov_b64 exec, s[50:51]\n\t" \
    "ds_read_b128 v[28:31], v1 offset:47360\n\t" \
    "s_mov_b64 exec, -1\n\t" \
    "v_exp_f32 v44, v44\n\t" \
    "v_exp_f32 v45, v45\n\t" \
    "v_exp_f32 v46, v46\n\t" \
    "v_exp_f32 v47, v47\n\t" \
    "v_smfmac_f32_32x32x32_bf16 v[104:119], v[20:23], v[36:43], v2\n\t" \
    "s_mov_b64 exec, s[50:51]\n\t" \
    "ds_read_b128 v[32:35], v1 offset:48000\n\t" \
    "s_mov_b64 exec, -1\n\t" \
    "v_exp_f32 v48, v48\n\t" \
    "v_exp_f32 v49, v49\n\t" \
    "v_exp_f32 v50, v50\n\t" \
    "v_exp_f32 v51, v51\n\t" \
    "v_mfma_f32_32x32x16_bf16 v[68:83], v[8:11], v[136:139], 0\n\t" \
    "s_cmp_eq_u32 s54, 1\n\t" \
    "v_exp_f32 v52, v52\n\t" \
    "v_exp_f32 v53, v53\n\t" \
    "v_exp_f32 v54, v54\n\t" \
    "v_exp_f32 v55, v55\n\t" \
    "v_mfma_f32_32x32x8_bf16 v[68:83], v[18:19], v[144:145], v[68:83]\n\t" \
    "v_exp_f32 v56, v56\n\t" \
    "v_exp_f32 v57, v57\n\t" \
    "v_exp_f32 v58, v58\n\t" \
    "v_exp_f32 v59, v59\n\t" \
    "v_smfmac_f32_32x32x32_bf16 v[104:119], v[24:27], v[44:51], v2\n\t" \
    "s_cbranch_scc0 NB_%=\n\t" \
    "s_barrier\n\t" \
    "NB_%=:\n\t" \
    "ds_read_b128 v[4:7], v0 offset:51200\n\t" \
    "v_exp_f32 v60, v60\n\t" \
    "v_exp_f32 v61, v61\n\t" \
    "v_exp_f32 v62, v62\n\t" \
    "v_exp_f32 v63, v63\n\t" \
    "v_smfmac_f32_32x32x32_bf16 v[120:135], v[20:23], v[52:59], v2\n\t" \
    "s_mov_b64 exec, s[52:53]\n\t" \
    "ds_read_b128 v[12:15], v0 offset:53248\n\t" \
    "s_mov_b64 exec, -1\n\t" \
    "v_exp_f32 v64, v64\n\t" \
    "v_exp_f32 v65, v65\n\t" \
    "v_exp_f32 v66, v66\n\t" \
    "v_exp_f32 v67, v67\n\t" \
    "v_mfma_f32_32x32x16_bf16 v[84:99], v[8:11], v[140:143], 0\n\t" \
    "v_exp_f32 v68, v68\n\t" \
    "v_exp_f32 v69, v69\n\t" \
    "v_exp_f32 v70, v70\n\t" \
    "v_exp_f32 v71, v71\n\t" \
    "v_mfma_f32_32x32x8_bf16 v[84:99], v[18:19], v[146:147], v[84:99]\n\t" \
    "v_exp_f32 v72, v72\n\t" \
    "v_exp_f32 v73, v73\n\t" \
    "v_exp_f32 v74, v74\n\t" \
    "v_exp_f32 v75, v75\n\t" \
    "v_smfmac_f32_32x32x32_bf16 v[120:135], v[24:27], v[60:67], v2\n\t" \
    "s_waitcnt lgkmcnt(0)\n\t" \
    "s_mov_b64 exec, s[50:51]\n\t" \
    "ds_read_b128 v[20:23], v1 offset:51200\n\t" \
    "s_mov_b64 exec, -1\n\t" \
    "v_exp_f32 v76, v76\n\t" \
    "v_exp_f32 v77, v77\n\t" \
    "v_exp_f32 v78, v78\n\t" \
    "v_exp_f32 v79, v79\n\t" \
    "v_smfmac_f32_32x32x32_bf16 v[104:119], v[28:31], v[68:75], v2\n\t" \
    "s_mov_b64 exec, s[50:51]\n\t" \
    "ds_read_b128 v[24:27], v1 offset:51840\n\t" \
    "s_mov_b64 exec, -1\n\t" \
    "v_exp_f32 v80, v80\n\t" \
    "v_exp_f32 v81, v81\n\t" \
    "v_exp_f32 v82, v82\n\t" \
    "v_exp_f32 v83, v83\n\t" \
    "v_mfma_f32_32x32x16_bf16 v[36:51], v[4:7], v[136:139], 0\n\t" \
    "v_exp_f32 v84, v84\n\t" \
    "v_exp_f32 v85, v85\n\t" \
    "v_exp_f32 v86, v86\n\t" \
    "v_exp_f32 v87, v87\n\t" \
    "v_mfma_f32_32x32x8_bf16 v[36:51], v[12:13], v[144:145], v[36:51]\n\t" \
    "v_exp_f32 v88, v88\n\t" \
    "v_exp_f32 v89, v89\n\t" \
    "v_exp_f32 v90, v90\n\t" \
    "v_exp_f32 v91, v91\n\t" \
    "v_smfmac_f32_32x32x32_bf16 v[104:119], v[32:35], v[76:83], v2\n\t" \
    "ds_read_b128 v[8:11], v0 offset:52224\n\t" \
    "v_exp_f32 v92, v92\n\t" \
    "v_exp_f32 v93, v93\n\t" \
    "v_exp_f32 v94, v94\n\t" \
    "v_exp_f32 v95, v95\n\t" \
    "v_smfmac_f32_32x32x32_bf16 v[120:135], v[28:31], v[84:91], v2\n\t" \
    "v_exp_f32 v96, v96\n\t" \
    "v_exp_f32 v97, v97\n\t" \
    "v_exp_f32 v98, v98\n\t" \
    "v_exp_f32 v99, v99\n\t" \
    "v_mfma_f32_32x32x16_bf16 v[52:67], v[4:7], v[140:143], 0\n\t" \
    "v_exp_f32 v36, v36\n\t" \
    "v_exp_f32 v37, v37\n\t" \
    "v_exp_f32 v38, v38\n\t" \
    "v_exp_f32 v39, v39\n\t" \
    "v_mfma_f32_32x32x8_bf16 v[52:67], v[12:13], v[146:147], v[52:67]\n\t" \
    "v_exp_f32 v40, v40\n\t" \
    "v_exp_f32 v41, v41\n\t" \
    "v_exp_f32 v42, v42\n\t" \
    "v_exp_f32 v43, v43\n\t" \
    "v_smfmac_f32_32x32x32_bf16 v[120:135], v[32:35], v[92:99], v2\n\t" \
    "s_waitcnt lgkmcnt(0)\n\t" \
    "s_mov_b64 exec, s[50:51]\n\t" \
    "ds_read_b128 v[28:31], v1 offset:52480\n\t" \
    "s_mov_b64 exec, -1\n\t" \
    "v_exp_f32 v44, v44\n\t" \
    "v_exp_f32 v45, v45\n\t" \
    "v_exp_f32 v46, v46\n\t" \
    "v_exp_f32 v47, v47\n\t" \
    "v_smfmac_f32_32x32x32_bf16 v[104:119], v[20:23], v[36:43], v2\n\t" \
    "s_mov_b64 exec, s[50:51]\n\t" \
    "ds_read_b128 v[32:35], v1 offset:53120\n\t" \
    "s_mov_b64 exec, -1\n\t" \
    "v_exp_f32 v48, v48\n\t" \
    "v_exp_f32 v49, v49\n\t" \
    "v_exp_f32 v50, v50\n\t" \
    "v_exp_f32 v51, v51\n\t" \
    "v_mfma_f32_32x32x16_bf16 v[68:83], v[8:11], v[136:139], 0\n\t" \
    "v_exp_f32 v52, v52\n\t" \
    "v_exp_f32 v53, v53\n\t" \
    "v_exp_f32 v54, v54\n\t" \
    "v_exp_f32 v55, v55\n\t" \
    "v_mfma_f32_32x32x8_bf16 v[68:83], v[14:15], v[144:145], v[68:83]\n\t" \
    "v_exp_f32 v56, v56\n\t" \
    "v_exp_f32 v57, v57\n\t" \
    "v_exp_f32 v58, v58\n\t" \
    "v_exp_f32 v59, v59\n\t" \
    "v_smfmac_f32_32x32x32_bf16 v[104:119], v[24:27], v[44:51], v2\n\t" \
    "ds_read_b128 v[4:7], v0 offset:56320\n\t" \
    "v_exp_f32 v60, v60\n\t" \
    "v_exp_f32 v61, v61\n\t" \
    "v_exp_f32 v62, v62\n\t" \
    "v_exp_f32 v63, v63\n\t" \
    "v_smfmac_f32_32x32x32_bf16 v[120:135], v[20:23], v[52:59], v2\n\t" \
    "s_mov_b64 exec, s[52:53]\n\t" \
    "ds_read_b128 v[16:19], v0 offset:58368\n\t" \
    "s_mov_b64 exec, -1\n\t" \
    "v_exp_f32 v64, v64\n\t" \
    "v_exp_f32 v65, v65\n\t" \
    "v_exp_f32 v66, v66\n\t" \
    "v_exp_f32 v67, v67\n\t" \
    "v_mfma_f32_32x32x16_bf16 v[84:99], v[8:11], v[140:143], 0\n\t" \
    "v_exp_f32 v68, v68\n\t" \
    "v_exp_f32 v69, v69\n\t" \
    "v_exp_f32 v70, v70\n\t" \
    "v_exp_f32 v71, v71\n\t" \
    "v_mfma_f32_32x32x8_bf16 v[84:99], v[14:15], v[146:147], v[84:99]\n\t" \
    "v_exp_f32 v72, v72\n\t" \
    "v_exp_f32 v73, v73\n\t" \
    "v_exp_f32 v74, v74\n\t" \
    "v_exp_f32 v75, v75\n\t" \
    "v_smfmac_f32_32x32x32_bf16 v[120:135], v[24:27], v[60:67], v2\n\t" \
    "s_waitcnt lgkmcnt(0)\n\t" \
    "s_mov_b64 exec, s[50:51]\n\t" \
    "ds_read_b128 v[20:23], v1 offset:56320\n\t" \
    "s_mov_b64 exec, -1\n\t" \
    "v_exp_f32 v76, v76\n\t" \
    "v_exp_f32 v77, v77\n\t" \
    "v_exp_f32 v78, v78\n\t" \
    "v_exp_f32 v79, v79\n\t" \
    "v_smfmac_f32_32x32x32_bf16 v[104:119], v[28:31], v[68:75], v2\n\t" \
    "s_mov_b64 exec, s[50:51]\n\t" \
    "ds_read_b128 v[24:27], v1 offset:56960\n\t" \
    "s_mov_b64 exec, -1\n\t" \
    "v_exp_f32 v80, v80\n\t" \
    "v_exp_f32 v81, v81\n\t" \
    "v_exp_f32 v82, v82\n\t" \
    "v_exp_f32 v83, v83\n\t" \
    "v_mfma_f32_32x32x16_bf16 v[36:51], v[4:7], v[136:139], 0\n\t" \
    "v_exp_f32 v84, v84\n\t" \
    "v_exp_f32 v85, v85\n\t" \
    "v_exp_f32 v86, v86\n\t" \
    "v_exp_f32 v87, v87\n\t" \
    "v_mfma_f32_32x32x8_bf16 v[36:51], v[16:17], v[144:145], v[36:51]\n\t" \
    "v_exp_f32 v88, v88\n\t" \
    "v_exp_f32 v89, v89\n\t" \
    "v_exp_f32 v90, v90\n\t" \
    "v_exp_f32 v91, v91\n\t" \
    "v_smfmac_f32_32x32x32_bf16 v[104:119], v[32:35], v[76:83], v2\n\t" \
    "ds_read_b128 v[8:11], v0 offset:57344\n\t" \
    "v_exp_f32 v92, v92\n\t" \
    "v_exp_f32 v93, v93\n\t" \
    "v_exp_f32 v94, v94\n\t" \
    "v_exp_f32 v95, v95\n\t" \
    "v_smfmac_f32_32x32x32_bf16 v[120:135], v[28:31], v[84:91], v2\n\t" \
    "v_exp_f32 v96, v96\n\t" \
    "v_exp_f32 v97, v97\n\t" \
    "v_exp_f32 v98, v98\n\t" \
    "v_exp_f32 v99, v99\n\t" \
    "v_mfma_f32_32x32x16_bf16 v[52:67], v[4:7], v[140:143], 0\n\t" \
    "v_exp_f32 v36, v36\n\t" \
    "v_exp_f32 v37, v37\n\t" \
    "v_exp_f32 v38, v38\n\t" \
    "v_exp_f32 v39, v39\n\t" \
    "v_mfma_f32_32x32x8_bf16 v[52:67], v[16:17], v[146:147], v[52:67]\n\t" \
    "v_exp_f32 v40, v40\n\t" \
    "v_exp_f32 v41, v41\n\t" \
    "v_exp_f32 v42, v42\n\t" \
    "v_exp_f32 v43, v43\n\t" \
    "v_smfmac_f32_32x32x32_bf16 v[120:135], v[32:35], v[92:99], v2\n\t" \
    "s_waitcnt lgkmcnt(0)\n\t" \
    "s_mov_b64 exec, s[50:51]\n\t" \
    "ds_read_b128 v[28:31], v1 offset:57600\n\t" \
    "s_mov_b64 exec, -1\n\t" \
    "v_exp_f32 v44, v44\n\t" \
    "v_exp_f32 v45, v45\n\t" \
    "v_exp_f32 v46, v46\n\t" \
    "v_exp_f32 v47, v47\n\t" \
    "v_smfmac_f32_32x32x32_bf16 v[104:119], v[20:23], v[36:43], v2\n\t" \
    "s_mov_b64 exec, s[50:51]\n\t" \
    "ds_read_b128 v[32:35], v1 offset:58240\n\t" \
    "s_mov_b64 exec, -1\n\t" \
    "v_exp_f32 v48, v48\n\t" \
    "v_exp_f32 v49, v49\n\t" \
    "v_exp_f32 v50, v50\n\t" \
    "v_exp_f32 v51, v51\n\t" \
    "v_mfma_f32_32x32x16_bf16 v[68:83], v[8:11], v[136:139], 0\n\t" \
    "v_exp_f32 v52, v52\n\t" \
    "v_exp_f32 v53, v53\n\t" \
    "v_exp_f32 v54, v54\n\t" \
    "v_exp_f32 v55, v55\n\t" \
    "v_mfma_f32_32x32x8_bf16 v[68:83], v[18:19], v[144:145], v[68:83]\n\t" \
    "v_exp_f32 v56, v56\n\t" \
    "v_exp_f32 v57, v57\n\t" \
    "v_exp_f32 v58, v58\n\t" \
    "v_exp_f32 v59, v59\n\t" \
    "v_smfmac_f32_32x32x32_bf16 v[104:119], v[24:27], v[44:51], v2\n\t" \
    "s_waitcnt lgkmcnt(0)\n\t" \
    "s_barrier\n\t" \
    "ds_read_b128 v[4:7], v0 offset:0\n\t" \
    "v_exp_f32 v60, v60\n\t" \
    "v_exp_f32 v61, v61\n\t" \
    "v_exp_f32 v62, v62\n\t" \
    "v_exp_f32 v63, v63\n\t" \
    "v_smfmac_f32_32x32x32_bf16 v[120:135], v[20:23], v[52:59], v2\n\t" \
    "s_mov_b64 exec, s[52:53]\n\t" \
    "ds_read_b128 v[12:15], v0 offset:2048\n\t" \
    "s_mov_b64 exec, -1\n\t" \
    "v_exp_f32 v64, v64\n\t" \
    "v_exp_f32 v65, v65\n\t" \
    "v_exp_f32 v66, v66\n\t" \
    "v_exp_f32 v67, v67\n\t" \
    "v_mfma_f32_32x32x16_bf16 v[84:99], v[8:11], v[140:143], 0\n\t" \
    "v_exp_f32 v68, v68\n\t" \
    "v_exp_f32 v69, v69\n\t" \
    "v_exp_f32 v70, v70\n\t" \
    "v_exp_f32 v71, v71\n\t" \
    "v_mfma_f32_32x32x8_bf16 v[84:99], v[18:19], v[146:147], v[84:99]\n\t" \
    "v_exp_f32 v72, v72\n\t" \
    "v_exp_f32 v73, v73\n\t" \
    "v_exp_f32 v74, v74\n\t" \
    "v_exp_f32 v75, v75\n\t" \
    "v_smfmac_f32_32x32x32_bf16 v[120:135], v[24:27], v[60:67], v2\n\t" \
    "s_waitcnt lgkmcnt(0)\n\t" \
    "s_mov_b64 exec, s[50:51]\n\t" \
    "ds_read_b128 v[20:23], v1 offset:0\n\t" \
    "s_mov_b64 exec, -1\n\t" \
    "v_exp_f32 v76, v76\n\t" \
    "v_exp_f32 v77, v77\n\t" \
    "v_exp_f32 v78, v78\n\t" \
    "v_exp_f32 v79, v79\n\t" \
    "v_smfmac_f32_32x32x32_bf16 v[104:119], v[28:31], v[68:75], v2\n\t" \
    "s_mov_b64 exec, s[50:51]\n\t" \
    "ds_read_b128 v[24:27], v1 offset:640\n\t" \
    "s_mov_b64 exec, -1\n\t" \
    "v_exp_f32 v80, v80\n\t" \
    "v_exp_f32 v81, v81\n\t" \
    "v_exp_f32 v82, v82\n\t" \
    "v_exp_f32 v83, v83\n\t" \
    "v_mfma_f32_32x32x16_bf16 v[36:51], v[4:7], v[136:139], 0\n\t" \
    "v_exp_f32 v84, v84\n\t" \
    "v_exp_f32 v85, v85\n\t" \
    "v_exp_f32 v86, v86\n\t" \
    "v_exp_f32 v87, v87\n\t" \
    "v_mfma_f32_32x32x8_bf16 v[36:51], v[12:13], v[144:145], v[36:51]\n\t" \
    "v_exp_f32 v88, v88\n\t" \
    "v_exp_f32 v89, v89\n\t" \
    "v_exp_f32 v90, v90\n\t" \
    "v_exp_f32 v91, v91\n\t" \
    "v_smfmac_f32_32x32x32_bf16 v[104:119], v[32:35], v[76:83], v2\n\t" \
    "ds_read_b128 v[8:11], v0 offset:1024\n\t" \
    "v_exp_f32 v92, v92\n\t" \
    "v_exp_f32 v93, v93\n\t" \
    "v_exp_f32 v94, v94\n\t" \
    "v_exp_f32 v95, v95\n\t" \
    "v_smfmac_f32_32x32x32_bf16 v[120:135], v[28:31], v[84:91], v2\n\t" \
    "s_add_u32 s54, s54, 1\n\t" \
    "s_cmp_lt_u32 s54, 8\n\t" \
    "v_exp_f32 v96, v96\n\t" \
    "v_exp_f32 v97, v97\n\t" \
    "v_exp_f32 v98, v98\n\t" \
    "v_exp_f32 v99, v99\n\t" \
    "s_cbranch_scc1 LOOP_%=\n\t" \
    "s_nop 0\n\t" \
    "v_smfmac_f32_32x32x32_bf16 v[120:135], v[32:35], v[92:99], v2\n\t" \
    "s_setprio 0\n\t" \
    "buffer_load_dword v157, v156, s[36:39], 0 offen sc1\n\t" \
    "s_mov_b32 s54, 0x35800000\n\t" \
    "s_mov_b32 s55, 0x76800000\n\t" \
    "s_waitcnt lgkmcnt(0)\n\t" \
    "v_pk_mul_f32 v[36:37], v[104:105], 0 op_sel_hi:[1,0]\n\t" \
    "v_pk_fma_f32 v[36:37], v[106:107], 0, v[36:37] op_sel_hi:[1,0,1]\n\t" \
    "v_pk_fma_f32 v[36:37], v[108:109], 0, v[36:37] op_sel_hi:[1,0,1]\n\t" \
    "v_pk_fma_f32 v[36:37], v[110:111], 0, v[36:37] op_sel_hi:[1,0,1]\n\t" \
    "v_pk_fma_f32 v[36:37], v[112:113], 0, v[36:37] op_sel_hi:[1,0,1]\n\t" \
    "v_pk_fma_f32 v[36:37], v[114:115], 0, v[36:37] op_sel_hi:[1,0,1]\n\t" \
    "v_cndmask_b32 v38, v112, v116, s[52:53]\n\t" \
    "s_nop 0\n\t" \
    "v_pk_fma_f32 v[36:37], v[120:121], 0, v[36:37] op_sel_hi:[1,0,1]\n\t" \
    "v_pk_fma_f32 v[36:37], v[122:123], 0, v[36:37] op_sel_hi:[1,0,1]\n\t" \
    "v_pk_fma_f32 v[36:37], v[124:125], 0, v[36:37] op_sel_hi:[1,0,1]\n\t" \
    "v_pk_fma_f32 v[36:37], v[126:127], 0, v[36:37] op_sel_hi:[1,0,1]\n\t" \
    "v_pk_fma_f32 v[36:37], v[128:129], 0, v[36:37] op_sel_hi:[1,0,1]\n\t" \
    "v_pk_fma_f32 v[36:37], v[130:131], 0, v[36:37] op_sel_hi:[1,0,1]\n\t" \
    "v_cndmask_b32 v39, v128, v132, s[52:53]\n\t" \
    "v_add_f32 v36, v36, v37\n\t" \
    "v_min_f32 v37, v38, v39\n\t" \
    "v_max_f32 v38, v38, v39\n\t" \
    "v_add_f32 v37, v36, v37\n\t" \
    "v_cmp_nle_f32 vcc, s54, v37\n\t" \
    "v_cmp_nge_f32 s[56:57], s55, v38\n\t" \
    "s_nop 1\n\t" \
    "s_or_b64 s[56:57], s[56:57], vcc\n\t" \
    : "+{v[104:119]}"(o[0]), "+{v[120:135]}"(o[1]), "+{v[144:145]}"(qr[0]), "+{v[146:147]}"(qr[1]), "+{v[8:11]}"(km0), "+{v[18:19]}"(kr0), "={s[56:57]}"(bad), "={v157}"(probe) \
    : "{v[136:139]}"(qm[0]), "{v[140:143]}"(qm[1]), "{v156}"(vP), "{s[36:39]}"(osrd), "{v150}"(aK0), "{v151}"(aV0), "{v152}"(ones), "{s[50:51]}"(vmask), "{s[52:53]}"(lomask), "{s58}"(wave), "{s59}"(hw), "{v[148:149]}"(ost), "{s[68:71]}"(wsrd) \
    : "v0", "v1", "v2", "v3", "v4", "v5", "v6", "v7", "v12", "v13", "v14", "v15", "v16", "v17", "v20", "v21", "v22", "v23", "v24", "v25", "v26", "v27", "v28", "v29", "v30", "v31", "v32", "v33", "v34", "v35", "v36", "v37", "v38", "v39", "v40", "v41", "v42", "v43", "v44", "v45", "v46", "v47", "v48", "v49", "v50", "v51", "v52", "v53", "v54", "v55", "v56", "v57", "v58", "v59", "v60", "v61", "v62", "v63", "v64", "v65", "v66", "v67", "v68", "v69", "v70", "v71", "v72", "v73", "v74", "v75", "v76", "v77", "v78", "v79", "v80", "v81", "v82", "v83", "v84", "v85", "v86", "v87", "v88", "v89", "v90", "v91", "v92", "v93", "v94", "v95", "v96", "v97", "v98", "v99", "s54", "s55", "vcc", "scc", "memory")

#define PUBLISH_O(o, probe, ost, wsrd, osrd, hw, lomask) \
  asm volatile( \
    "v_cndmask_b32 v0, v112, v116, s[52:53]\n\t" \
    "v_cndmask_b32 v2, v128, v132, s[52:53]\n\t" \
    "v_rcp_f32 v0, v0\n\t" \
    "v_rcp_f32 v2, v2\n\t" \
    "v_mov_b32 v4, 0\n\t" \
    "s_mov_b32 s46, 40960\n\t" \
    "v_pk_mul_f32 v[104:105], v[104:105], v[0:1] op_sel_hi:[1,0]\n\t" \
    "v_pk_mul_f32 v[106:107], v[106:107], v[0:1] op_sel_hi:[1,0]\n\t" \
    "v_pk_mul_f32 v[108:109], v[108:109], v[0:1] op_sel_hi:[1,0]\n\t" \
    "v_pk_mul_f32 v[110:111], v[110:111], v[0:1] op_sel_hi:[1,0]\n\t" \
    "v_cvt_pk_bf16_f32 v8, v104, v105\n\t" \
    "v_cvt_pk_bf16_f32 v9, v106, v107\n\t" \
    "v_cvt_pk_bf16_f32 v10, v108, v109\n\t" \
    "v_cvt_pk_bf16_f32 v11, v110, v111\n\t" \
    "v_min_u32 v8, -2, v8\n\t" \
    "v_min_u32 v9, -2, v9\n\t" \
    "v_min_u32 v10, -2, v10\n\t" \
    "v_min_u32 v11, -2, v11\n\t" \
    "buffer_store_dwordx4 v[8:11], v148, s[68:71], 0 offen sc1\n\t" \
    "v_pk_mul_f32 v[112:113], v[112:113], v[0:1] op_sel_hi:[1,0]\n\t" \
    "v_pk_mul_f32 v[114:115], v[114:115], v[0:1] op_sel_hi:[1,0]\n\t" \
    "v_cvt_pk_bf16_f32 v12, v112, v113\n\t" \
    "v_cvt_pk_bf16_f32 v13, v114, v115\n\t" \
    "v_min_u32 v12, -2, v12\n\t" \
    "v_min_u32 v13, -2, v13\n\t" \
    "s_mov_b64 exec, s[52:53]\n\t" \
    "buffer_store_dwordx2 v[12:13], v149, s[68:71], 0 offen sc1\n\t" \
    "s_mov_b64 exec, -1\n\t" \
    "v_pk_mul_f32 v[120:121], v[120:121], v[2:3] op_sel_hi:[1,0]\n\t" \
    "v_pk_mul_f32 v[122:123], v[122:123], v[2:3] op_sel_hi:[1,0]\n\t" \
    "v_pk_mul_f32 v[124:125], v[124:125], v[2:3] op_sel_hi:[1,0]\n\t" \
    "v_pk_mul_f32 v[126:127], v[126:127], v[2:3] op_sel_hi:[1,0]\n\t" \
    "v_cvt_pk_bf16_f32 v16, v120, v121\n\t" \
    "v_cvt_pk_bf16_f32 v17, v122, v123\n\t" \
    "v_cvt_pk_bf16_f32 v18, v124, v125\n\t" \
    "v_cvt_pk_bf16_f32 v19, v126, v127\n\t" \
    "v_min_u32 v16, -2, v16\n\t" \
    "v_min_u32 v17, -2, v17\n\t" \
    "v_min_u32 v18, -2, v18\n\t" \
    "v_min_u32 v19, -2, v19\n\t" \
    "buffer_store_dwordx4 v[16:19], v148, s[68:71], s46 offen sc1\n\t" \
    "v_pk_mul_f32 v[128:129], v[128:129], v[2:3] op_sel_hi:[1,0]\n\t" \
    "v_pk_mul_f32 v[130:131], v[130:131], v[2:3] op_sel_hi:[1,0]\n\t" \
    "v_cvt_pk_bf16_f32 v20, v128, v129\n\t" \
    "v_cvt_pk_bf16_f32 v21, v130, v131\n\t" \
    "v_min_u32 v20, -2, v20\n\t" \
    "v_min_u32 v21, -2, v21\n\t" \
    "s_mov_b64 exec, s[52:53]\n\t" \
    "buffer_store_dwordx2 v[20:21], v149, s[68:71], s46 offen sc1\n\t" \
    "s_mov_b64 exec, -1\n\t" \
    "s_cmp_eq_u32 s59, 0\n\t" \
    "s_barrier\n\t" \
    "s_cbranch_scc1 ONH_%=\n\t" \
    "s_mov_b64 exec, 1\n\t" \
    "buffer_store_dword v4, off, s[36:39], s59 sc1\n\t" \
    "s_mov_b64 exec, -1\n\t" \
    "s_waitcnt vmcnt(5)\n\t" \
    "s_branch OHD_%=\n\t" \
    "ONH_%=:\n\t" \
    "s_waitcnt vmcnt(4)\n\t" \
    "OHD_%=:\n\t" \
    : "+{v[104:119]}"(o[0]), "+{v[120:135]}"(o[1]), "+{v157}"(probe) \
    : "{v[148:149]}"(ost), "{s[68:71]}"(wsrd), "{s[36:39]}"(osrd), "{s59}"(hw), "{s[52:53]}"(lomask) \
    : "v0", "v1", "v2", "v3", "v4", "v5", "v6", "v7", "v8", "v9", "v10", "v11", "v12", "v13", "v14", "v15", "v16", "v17", "v18", "v19", "v20", "v21", "v22", "v23", "s46", "scc", "memory")

#define OPROJ_CORE(vO, vO2, wb, vP, vS, xa, da, probe, osrd, ko, ld, outsrd) \
  asm volatile( \
    "v_cmp_eq_u32 vcc, -1, v157\n\t" \
    "ds_read_b64_tr_b16 v[48:49], v4 offset:0\n\t" \
    "ds_read_b64_tr_b16 v[50:51], v4 offset:2560\n\t" \
    "ds_read_b64_tr_b16 v[52:53], v4 offset:32\n\t" \
    "ds_read_b64_tr_b16 v[54:55], v4 offset:2592\n\t" \
    "ds_read_b64_tr_b16 v[56:57], v4 offset:64\n\t" \
    "ds_read_b64_tr_b16 v[58:59], v4 offset:2624\n\t" \
    "ds_read_b64_tr_b16 v[60:61], v4 offset:96\n\t" \
    "ds_read_b64_tr_b16 v[62:63], v4 offset:2656\n\t" \
    "ds_read_b64_tr_b16 v[64:65], v4 offset:128\n\t" \
    "ds_read_b64_tr_b16 v[66:67], v4 offset:2688\n\t" \
    "s_mov_b32 s44, 200000\n\t" \
    "s_and_b64 vcc, exec, vcc\n\t" \
    "s_cbranch_vccz DZ2_%=\n\t" \
    "s_cmp_eq_u32 s45, 0\n\t" \
    "s_cbranch_scc1 DZ1_%=\n\t" \
    "DZ0_%=:\n\t" \
    "ds_read_b32 v157, v7\n\t" \
    "s_cmp_eq_u32 s44, 0\n\t" \
    "s_cbranch_scc1 OR0_%=\n\t" \
    "s_sub_u32 s44, s44, 1\n\t" \
    "s_waitcnt lgkmcnt(0)\n\t" \
    "v_cmp_eq_u32 vcc, 0, v157\n\t" \
    "s_and_b64 vcc, exec, vcc\n\t" \
    "s_cbranch_vccz OR0_%=\n\t" \
    "s_sleep 1\n\t" \
    "s_branch DZ0_%=\n\t" \
    "DZ1_%=:\n\t" \
    "s_sleep 3\n\t" \
    "buffer_load_dword v157, v156, s[36:39], 0 offen sc1\n\t" \
    "s_cmp_eq_u32 s44, 0\n\t" \
    "s_cbranch_scc1 DZ2_%=\n\t" \
    "s_sub_u32 s44, s44, 1\n\t" \
    "s_waitcnt vmcnt(0)\n\t" \
    "v_cmp_eq_u32 vcc, -1, v157\n\t" \
    "s_and_b64 vcc, exec, vcc\n\t" \
    "s_cbranch_vccnz DZ1_%=\n\t" \
    "DZ2_%=:\n\t" \
    "v_mov_b32 v8, 1\n\t" \
    "ds_write_b32 v7, v8\n\t" \
    "s_waitcnt lgkmcnt(0)\n\t" \
    "OR0_%=:\n\t" \
    "buffer_load_dwordx4 v[72:75], v0, s[36:39], s40 offen offset:0 sc1\n\t" \
    "buffer_load_dwordx4 v[76:79], v1, s[36:39], s40 offen offset:0 sc1\n\t" \
    "buffer_load_dwordx4 v[80:83], v0, s[36:39], s40 offen offset:1024 sc1\n\t" \
    "buffer_load_dwordx4 v[84:87], v1, s[36:39], s40 offen offset:1024 sc1\n\t" \
    "buffer_load_dwordx4 v[88:91], v0, s[36:39], s40 offen offset:2048 sc1\n\t" \
    "buffer_load_dwordx4 v[92:95], v1, s[36:39], s40 offen offset:2048 sc1\n\t" \
    "s_waitcnt vmcnt(4)\n\t" \
    "OC0_%=:\n\t" \
    "v_max3_u32 v6, v72, v73, v74\n\t" \
    "v_max3_u32 v6, v6, v75, v76\n\t" \
    "v_max3_u32 v6, v6, v77, v78\n\t" \
    "v_max_u32 v6, v6, v79\n\t" \
    "v_cmp_eq_u32 vcc, -1, v6\n\t" \
    "s_and_b64 vcc, exec, vcc\n\t" \
    "s_cbranch_vccnz OF0_%=\n\t" \
    "OK0_%=:\n\t" \
    "s_waitcnt lgkmcnt(8)\n\t" \
    "v_mfma_f32_16x16x32_bf16 v[8:11], v[48:51], v[72:75], 0\n\t" \
    "ds_read_b64_tr_b16 v[68:69], v4 offset:5120\n\t" \
    "ds_read_b64_tr_b16 v[70:71], v4 offset:7680\n\t" \
    "buffer_load_dwordx4 v[96:99], v0, s[36:39], s40 offen offset:3072 sc1\n\t" \
    "v_mfma_f32_16x16x32_bf16 v[28:31], v[48:51], v[76:79], 0\n\t" \
    "buffer_load_dwordx4 v[100:103], v1, s[36:39], s40 offen offset:3072 sc1\n\t" \
    "s_waitcnt lgkmcnt(8)\n\t" \
    "v_mfma_f32_16x16x32_bf16 v[12:15], v[52:55], v[72:75], 0\n\t" \
    "ds_read_b64_tr_b16 v[48:49], v4 offset:5152\n\t" \
    "ds_read_b64_tr_b16 v[50:51], v4 offset:7712\n\t" \
    "v_mfma_f32_16x16x32_bf16 v[32:35], v[52:55], v[76:79], 0\n\t" \
    "s_waitcnt lgkmcnt(8)\n\t" \
    "v_mfma_f32_16x16x32_bf16 v[16:19], v[56:59], v[72:75], 0\n\t" \
    "ds_read_b64_tr_b16 v[52:53], v4 offset:5184\n\t" \
    "ds_read_b64_tr_b16 v[54:55], v4 offset:7744\n\t" \
    "v_mfma_f32_16x16x32_bf16 v[36:39], v[56:59], v[76:79], 0\n\t" \
    "s_waitcnt lgkmcnt(8)\n\t" \
    "v_mfma_f32_16x16x32_bf16 v[20:23], v[60:63], v[72:75], 0\n\t" \
    "ds_read_b64_tr_b16 v[56:57], v4 offset:5216\n\t" \
    "ds_read_b64_tr_b16 v[58:59], v4 offset:7776\n\t" \
    "v_mfma_f32_16x16x32_bf16 v[40:43], v[60:63], v[76:79], 0\n\t" \
    "s_waitcnt lgkmcnt(8)\n\t" \
    "v_mfma_f32_16x16x32_bf16 v[24:27], v[64:67], v[72:75], 0\n\t" \
    "ds_read_b64_tr_b16 v[60:61], v4 offset:5248\n\t" \
    "ds_read_b64_tr_b16 v[62:63], v4 offset:7808\n\t" \
    "v_mfma_f32_16x16x32_bf16 v[44:47], v[64:67], v[76:79], 0\n\t" \
    "s_waitcnt vmcnt(4)\n\t" \
    "OC1_%=:\n\t" \
    "v_max3_u32 v6, v80, v81, v82\n\t" \
    "v_max3_u32 v6, v6, v83, v84\n\t" \
    "v_max3_u32 v6, v6, v85, v86\n\t" \
    "v_max_u32 v6, v6, v87\n\t" \
    "v_cmp_eq_u32 vcc, -1, v6\n\t" \
    "s_and_b64 vcc, exec, vcc\n\t" \
    "s_cbranch_vccnz OF1_%=\n\t" \
    "OK1_%=:\n\t" \
    "s_waitcnt lgkmcnt(8)\n\t" \
    "v_mfma_f32_16x16x32_bf16 v[8:11], v[68:71], v[80:83], v[8:11]\n\t" \
    "ds_read_b64_tr_b16 v[64:65], v4 offset:10240\n\t" \
    "ds_read_b64_tr_b16 v[66:67], v4 offset:12800\n\t" \
    "buffer_load_dwordx4 v[104:107], v0, s[36:39], s41 offen offset:0 sc1\n\t" \
    "v_mfma_f32_16x16x32_bf16 v[28:31], v[68:71], v[84:87], v[28:31]\n\t" \
    "buffer_load_dwordx4 v[108:111], v1, s[36:39], s41 offen offset:0 sc1\n\t" \
    "s_waitcnt lgkmcnt(8)\n\t" \
    "v_mfma_f32_16x16x32_bf16 v[12:15], v[48:51], v[80:83], v[12:15]\n\t" \
    "ds_read_b64_tr_b16 v[68:69], v4 offset:10272\n\t" \
    "ds_read_b64_tr_b16 v[70:71], v4 offset:12832\n\t" \
    "v_mfma_f32_16x16x32_bf16 v[32:35], v[48:51], v[84:87], v[32:35]\n\t" \
    "s_waitcnt lgkmcnt(8)\n\t" \
    "v_mfma_f32_16x16x32_bf16 v[16:19], v[52:55], v[80:83], v[16:19]\n\t" \
    "ds_read_b64_tr_b16 v[48:49], v4 offset:10304\n\t" \
    "ds_read_b64_tr_b16 v[50:51], v4 offset:12864\n\t" \
    "v_mfma_f32_16x16x32_bf16 v[36:39], v[52:55], v[84:87], v[36:39]\n\t" \
    "s_waitcnt lgkmcnt(8)\n\t" \
    "v_mfma_f32_16x16x32_bf16 v[20:23], v[56:59], v[80:83], v[20:23]\n\t" \
    "ds_read_b64_tr_b16 v[52:53], v4 offset:10336\n\t" \
    "ds_read_b64_tr_b16 v[54:55], v4 offset:12896\n\t" \
    "v_mfma_f32_16x16x32_bf16 v[40:43], v[56:59], v[84:87], v[40:43]\n\t" \
    "s_waitcnt lgkmcnt(8)\n\t" \
    "v_mfma_f32_16x16x32_bf16 v[24:27], v[60:63], v[80:83], v[24:27]\n\t" \
    "ds_read_b64_tr_b16 v[56:57], v4 offset:10368\n\t" \
    "ds_read_b64_tr_b16 v[58:59], v4 offset:12928\n\t" \
    "v_mfma_f32_16x16x32_bf16 v[44:47], v[60:63], v[84:87], v[44:47]\n\t" \
    "s_waitcnt vmcnt(4)\n\t" \
    "OC2_%=:\n\t" \
    "v_max3_u32 v6, v88, v89, v90\n\t" \
    "v_max3_u32 v6, v6, v91, v92\n\t" \
    "v_max3_u32 v6, v6, v93, v94\n\t" \
    "v_max_u32 v6, v6, v95\n\t" \
    "v_cmp_eq_u32 vcc, -1, v6\n\t" \
    "s_and_b64 vcc, exec, vcc\n\t" \
    "s_cbranch_vccnz OF2_%=\n\t" \
    "OK2_%=:\n\t" \
    "s_waitcnt lgkmcnt(8)\n\t" \
    "v_mfma_f32_16x16x32_bf16 v[8:11], v[64:67], v[88:91], v[8:11]\n\t" \
    "ds_read_b64_tr_b16 v[60:61], v4 offset:15360\n\t" \
    "ds_read_b64_tr_b16 v[62:63], v4 offset:17920\n\t" \
    "buffer_load_dwordx4 v[112:115], v0, s[36:39], s41 offen offset:1024 sc1\n\t" \
    "v_mfma_f32_16x16x32_bf16 v[28:31], v[64:67], v[92:95], v[28:31]\n\t" \
    "buffer_load_dwordx4 v[116:119], v1, s[36:39], s41 offen offset:1024 sc1\n\t" \
    "s_waitcnt lgkmcnt(8)\n\t" \
    "v_mfma_f32_16x16x32_bf16 v[12:15], v[68:71], v[88:91], v[12:15]\n\t" \
    "ds_read_b64_tr_b16 v[64:65], v4 offset:15392\n\t" \
    "ds_read_b64_tr_b16 v[66:67], v4 offset:17952\n\t" \
    "v_mfma_f32_16x16x32_bf16 v[32:35], v[68:71], v[92:95], v[32:35]\n\t" \
    "s_waitcnt lgkmcnt(8)\n\t" \
    "v_mfma_f32_16x16x32_bf16 v[16:19], v[48:51], v[88:91], v[16:19]\n\t" \
    "ds_read_b64_tr_b16 v[68:69], v4 offset:15424\n\t" \
    "ds_read_b64_tr_b16 v[70:71], v4 offset:17984\n\t" \
    "v_mfma_f32_16x16x32_bf16 v[36:39], v[48:51], v[92:95], v[36:39]\n\t" \
    "s_waitcnt lgkmcnt(8)\n\t" \
    "v_mfma_f32_16x16x32_bf16 v[20:23], v[52:55], v[88:91], v[20:23]\n\t" \
    "ds_read_b64_tr_b16 v[48:49], v4 offset:15456\n\t" \
    "ds_read_b64_tr_b16 v[50:51], v4 offset:18016\n\t" \
    "v_mfma_f32_16x16x32_bf16 v[40:43], v[52:55], v[92:95], v[40:43]\n\t" \
    "s_waitcnt lgkmcnt(8)\n\t" \
    "v_mfma_f32_16x16x32_bf16 v[24:27], v[56:59], v[88:91], v[24:27]\n\t" \
    "ds_read_b64_tr_b16 v[52:53], v4 offset:15488\n\t" \
    "ds_read_b64_tr_b16 v[54:55], v4 offset:18048\n\t" \
    "v_mfma_f32_16x16x32_bf16 v[44:47], v[56:59], v[92:95], v[44:47]\n\t" \
    "s_waitcnt vmcnt(4)\n\t" \
    "OC3_%=:\n\t" \
    "v_max3_u32 v6, v96, v97, v98\n\t" \
    "v_max3_u32 v6, v6, v99, v100\n\t" \
    "v_max3_u32 v6, v6, v101, v102\n\t" \
    "v_max_u32 v6, v6, v103\n\t" \
    "v_cmp_eq_u32 vcc, -1, v6\n\t" \
    "s_and_b64 vcc, exec, vcc\n\t" \
    "s_cbranch_vccnz OF3_%=\n\t" \
    "OK3_%=:\n\t" \
    "s_waitcnt lgkmcnt(8)\n\t" \
    "v_mfma_f32_16x16x32_bf16 v[8:11], v[60:63], v[96:99], v[8:11]\n\t" \
    "ds_read_b64_tr_b16 v[56:57], v4 offset:20480\n\t" \
    "ds_read_b64_tr_b16 v[58:59], v4 offset:23040\n\t" \
    "buffer_load_dwordx4 v[120:123], v0, s[36:39], s41 offen offset:2048 sc1\n\t" \
    "v_mfma_f32_16x16x32_bf16 v[28:31], v[60:63], v[100:103], v[28:31]\n\t" \
    "buffer_load_dwordx4 v[124:127], v1, s[36:39], s41 offen offset:2048 sc1\n\t" \
    "s_waitcnt lgkmcnt(8)\n\t" \
    "v_mfma_f32_16x16x32_bf16 v[12:15], v[64:67], v[96:99], v[12:15]\n\t" \
    "ds_read_b64_tr_b16 v[60:61], v4 offset:20512\n\t" \
    "ds_read_b64_tr_b16 v[62:63], v4 offset:23072\n\t" \
    "v_mfma_f32_16x16x32_bf16 v[32:35], v[64:67], v[100:103], v[32:35]\n\t" \
    "s_waitcnt lgkmcnt(8)\n\t" \
    "v_mfma_f32_16x16x32_bf16 v[16:19], v[68:71], v[96:99], v[16:19]\n\t" \
    "ds_read_b64_tr_b16 v[64:65], v4 offset:20544\n\t" \
    "ds_read_b64_tr_b16 v[66:67], v4 offset:23104\n\t" \
    "v_mfma_f32_16x16x32_bf16 v[36:39], v[68:71], v[100:103], v[36:39]\n\t" \
    "s_waitcnt lgkmcnt(8)\n\t" \
    "v_mfma_f32_16x16x32_bf16 v[20:23], v[48:51], v[96:99], v[20:23]\n\t" \
    "ds_read_b64_tr_b16 v[68:69], v4 offset:20576\n\t" \
    "ds_read_b64_tr_b16 v[70:71], v4 offset:23136\n\t" \
    "v_mfma_f32_16x16x32_bf16 v[40:43], v[48:51], v[100:103], v[40:43]\n\t" \
    "s_waitcnt lgkmcnt(8)\n\t" \
    "v_mfma_f32_16x16x32_bf16 v[24:27], v[52:55], v[96:99], v[24:27]\n\t" \
    "ds_read_b64_tr_b16 v[48:49], v4 offset:20608\n\t" \
    "ds_read_b64_tr_b16 v[50:51], v4 offset:23168\n\t" \
    "v_mfma_f32_16x16x32_bf16 v[44:47], v[52:55], v[100:103], v[44:47]\n\t" \
    "s_waitcnt vmcnt(4)\n\t" \
    "OC4_%=:\n\t" \
    "v_max3_u32 v6, v104, v105, v106\n\t" \
    "v_max3_u32 v6, v6, v107, v108\n\t" \
    "v_max3_u32 v6, v6, v109, v110\n\t" \
    "v_max_u32 v6, v6, v111\n\t" \
    "v_cmp_eq_u32 vcc, -1, v6\n\t" \
    "s_and_b64 vcc, exec, vcc\n\t" \
    "s_cbranch_vccnz OF4_%=\n\t" \
    "OK4_%=:\n\t" \
    "s_waitcnt lgkmcnt(8)\n\t" \
    "v_mfma_f32_16x16x32_bf16 v[8:11], v[56:59], v[104:107], v[8:11]\n\t" \
    "ds_read_b64_tr_b16 v[52:53], v4 offset:25600\n\t" \
    "ds_read_b64_tr_b16 v[54:55], v4 offset:28160\n\t" \
    "buffer_load_dwordx4 v[128:131], v0, s[36:39], s41 offen offset:3072 sc1\n\t" \
    "v_mfma_f32_16x16x32_bf16 v[28:31], v[56:59], v[108:111], v[28:31]\n\t" \
    "buffer_load_dwordx4 v[132:135], v1, s[36:39], s41 offen offset:3072 sc1\n\t" \
    "s_waitcnt lgkmcnt(8)\n\t" \
    "v_mfma_f32_16x16x32_bf16 v[12:15], v[60:63], v[104:107], v[12:15]\n\t" \
    "ds_read_b64_tr_b16 v[56:57], v4 offset:25632\n\t" \
    "ds_read_b64_tr_b16 v[58:59], v4 offset:28192\n\t" \
    "v_mfma_f32_16x16x32_bf16 v[32:35], v[60:63], v[108:111], v[32:35]\n\t" \
    "s_waitcnt lgkmcnt(8)\n\t" \
    "v_mfma_f32_16x16x32_bf16 v[16:19], v[64:67], v[104:107], v[16:19]\n\t" \
    "ds_read_b64_tr_b16 v[60:61], v4 offset:25664\n\t" \
    "ds_read_b64_tr_b16 v[62:63], v4 offset:28224\n\t" \
    "v_mfma_f32_16x16x32_bf16 v[36:39], v[64:67], v[108:111], v[36:39]\n\t" \
    "s_waitcnt lgkmcnt(8)\n\t" \
    "v_mfma_f32_16x16x32_bf16 v[20:23], v[68:71], v[104:107], v[20:23]\n\t" \
    "ds_read_b64_tr_b16 v[64:65], v4 offset:25696\n\t" \
    "ds_read_b64_tr_b16 v[66:67], v4 offset:28256\n\t" \
    "v_mfma_f32_16x16x32_bf16 v[40:43], v[68:71], v[108:111], v[40:43]\n\t" \
    "s_waitcnt lgkmcnt(8)\n\t" \
    "v_mfma_f32_16x16x32_bf16 v[24:27], v[48:51], v[104:107], v[24:27]\n\t" \
    "ds_read_b64_tr_b16 v[68:69], v4 offset:25728\n\t" \
    "ds_read_b64_tr_b16 v[70:71], v4 offset:28288\n\t" \
    "v_mfma_f32_16x16x32_bf16 v[44:47], v[48:51], v[108:111], v[44:47]\n\t" \
    "s_waitcnt vmcnt(4)\n\t" \
    "OC5_%=:\n\t" \
    "v_max3_u32 v6, v112, v113, v114\n\t" \
    "v_max3_u32 v6, v6, v115, v116\n\t" \
    "v_max3_u32 v6, v6, v117, v118\n\t" \
    "v_max_u32 v6, v6, v119\n\t" \
    "v_cmp_eq_u32 vcc, -1, v6\n\t" \
    "s_and_b64 vcc, exec, vcc\n\t" \
    "s_cbranch_vccnz OF5_%=\n\t" \
    "OK5_%=:\n\t" \
    "s_waitcnt lgkmcnt(8)\n\t" \
    "v_mfma_f32_16x16x32_bf16 v[8:11], v[52:55], v[112:115], v[8:11]\n\t" \
    "ds_read_b64_tr_b16 v[48:49], v4 offset:30720\n\t" \
    "ds_read_b64_tr_b16 v[50:51], v4 offset:33280\n\t" \
    "buffer_load_dwordx2 v[136:137], v2, s[36:39], s40 offen offset:0 sc1\n\t" \
    "v_mfma_f32_16x16x32_bf16 v[28:31], v[52:55], v[116:119], v[28:31]\n\t" \
    "buffer_load_dwordx2 v[138:139], v2, s[36:39], s40 offen offset:512 sc1\n\t" \
    "s_waitcnt lgkmcnt(8)\n\t" \
    "v_mfma_f32_16x16x32_bf16 v[12:15], v[56:59], v[112:115], v[12:15]\n\t" \
    "ds_read_b64_tr_b16 v[52:53], v4 offset:30752\n\t" \
    "ds_read_b64_tr_b16 v[54:55], v4 offset:33312\n\t" \
    "buffer_load_dwordx2 v[140:141], v3, s[36:39], s40 offen offset:0 sc1\n\t" \
    "v_mfma_f32_16x16x32_bf16 v[32:35], v[56:59], v[116:119], v[32:35]\n\t" \
    "buffer_load_dwordx2 v[142:143], v3, s[36:39], s40 offen offset:512 sc1\n\t" \
    "s_waitcnt lgkmcnt(8)\n\t" \
    "v_mfma_f32_16x16x32_bf16 v[16:19], v[60:63], v[112:115], v[16:19]\n\t" \
    "ds_read_b64_tr_b16 v[56:57], v4 offset:30784\n\t" \
    "ds_read_b64_tr_b16 v[58:59], v4 offset:33344\n\t" \
    "v_mfma_f32_16x16x32_bf16 v[36:39], v[60:63], v[116:119], v[36:39]\n\t" \
    "s_waitcnt lgkmcnt(8)\n\t" \
    "v_mfma_f32_16x16x32_bf16 v[20:23], v[64:67], v[112:115], v[20:23]\n\t" \
    "ds_read_b64_tr_b16 v[60:61], v4 offset:30816\n\t" \
    "ds_read_b64_tr_b16 v[62:63], v4 offset:33376\n\t" \
    "v_mfma_f32_16x16x32_bf16 v[40:43], v[64:67], v[116:119], v[40:43]\n\t" \
    "s_waitcnt lgkmcnt(8)\n\t" \
    "v_mfma_f32_16x16x32_bf16 v[24:27], v[68:71], v[112:115], v[24:27]\n\t" \
    "ds_read_b64_tr_b16 v[64:65], v4 offset:30848\n\t" \
    "ds_read_b64_tr_b16 v[66:67], v4 offset:33408\n\t" \
    "v_mfma_f32_16x16x32_bf16 v[44:47], v[68:71], v[116:119], v[44:47]\n\t" \
    "s_waitcnt vmcnt(6)\n\t" \
    "OC6_%=:\n\t" \
    "v_max3_u32 v6, v120, v121, v122\n\t" \
    "v_max3_u32 v6, v6, v123, v124\n\t" \
    "v_max3_u32 v6, v6, v125, v126\n\t" \
    "v_max_u32 v6, v6, v127\n\t" \
    "v_cmp_eq_u32 vcc, -1, v6\n\t" \
    "s_and_b64 vcc, exec, vcc\n\t" \
    "s_cbranch_vccnz OF6_%=\n\t" \
    "OK6_%=:\n\t" \
    "s_waitcnt lgkmcnt(8)\n\t" \
    "v_mfma_f32_16x16x32_bf16 v[8:11], v[48:51], v[120:123], v[8:11]\n\t" \
    "ds_read_b64_tr_b16 v[68:69], v4 offset:35840\n\t" \
    "ds_read_b64_tr_b16 v[70:71], v4 offset:38400\n\t" \
    "buffer_load_dwordx2 v[144:145], v2, s[36:39], s41 offen offset:0 sc1\n\t" \
    "v_mfma_f32_16x16x32_bf16 v[28:31], v[48:51], v[124:127], v[28:31]\n\t" \
    "buffer_load_dwordx2 v[146:147], v2, s[36:39], s41 offen offset:512 sc1\n\t" \
    "s_waitcnt lgkmcnt(8)\n\t" \
    "v_mfma_f32_16x16x32_bf16 v[12:15], v[52:55], v[120:123], v[12:15]\n\t" \
    "ds_read_b64_tr_b16 v[48:49], v4 offset:35872\n\t" \
    "ds_read_b64_tr_b16 v[50:51], v4 offset:38432\n\t" \
    "buffer_load_dwordx2 v[148:149], v3, s[36:39], s41 offen offset:0 sc1\n\t" \
    "v_mfma_f32_16x16x32_bf16 v[32:35], v[52:55], v[124:127], v[32:35]\n\t" \
    "buffer_load_dwordx2 v[150:151], v3, s[36:39], s41 offen offset:512 sc1\n\t" \
    "s_waitcnt lgkmcnt(8)\n\t" \
    "v_mfma_f32_16x16x32_bf16 v[16:19], v[56:59], v[120:123], v[16:19]\n\t" \
    "ds_read_b64_tr_b16 v[52:53], v4 offset:35904\n\t" \
    "ds_read_b64_tr_b16 v[54:55], v4 offset:38464\n\t" \
    "v_mfma_f32_16x16x32_bf16 v[36:39], v[56:59], v[124:127], v[36:39]\n\t" \
    "s_waitcnt lgkmcnt(8)\n\t" \
    "v_mfma_f32_16x16x32_bf16 v[20:23], v[60:63], v[120:123], v[20:23]\n\t" \
    "ds_read_b64_tr_b16 v[56:57], v4 offset:35936\n\t" \
    "ds_read_b64_tr_b16 v[58:59], v4 offset:38496\n\t" \
    "v_mfma_f32_16x16x32_bf16 v[40:43], v[60:63], v[124:127], v[40:43]\n\t" \
    "s_waitcnt lgkmcnt(8)\n\t" \
    "v_mfma_f32_16x16x32_bf16 v[24:27], v[64:67], v[120:123], v[24:27]\n\t" \
    "ds_read_b64_tr_b16 v[60:61], v4 offset:35968\n\t" \
    "ds_read_b64_tr_b16 v[62:63], v4 offset:38528\n\t" \
    "v_mfma_f32_16x16x32_bf16 v[44:47], v[64:67], v[124:127], v[44:47]\n\t" \
    "s_waitcnt vmcnt(8)\n\t" \
    "OC7_%=:\n\t" \
    "v_max3_u32 v6, v128, v129, v130\n\t" \
    "v_max3_u32 v6, v6, v131, v132\n\t" \
    "v_max3_u32 v6, v6, v133, v134\n\t" \
    "v_max_u32 v6, v6, v135\n\t" \
    "v_cmp_eq_u32 vcc, -1, v6\n\t" \
    "s_and_b64 vcc, exec, vcc\n\t" \
    "s_cbranch_vccnz OF7_%=\n\t" \
    "OK7_%=:\n\t" \
    "s_waitcnt lgkmcnt(8)\n\t" \
    "v_mfma_f32_16x16x32_bf16 v[8:11], v[68:71], v[128:131], v[8:11]\n\t" \
    "ds_read_b64_tr_b16 v[64:65], v5 offset:0\n\t" \
    "ds_read_b64_tr_b16 v[66:67], v5 offset:2560\n\t" \
    "v_mfma_f32_16x16x32_bf16 v[28:31], v[68:71], v[132:135], v[28:31]\n\t" \
    "s_waitcnt lgkmcnt(8)\n\t" \
    "v_mfma_f32_16x16x32_bf16 v[12:15], v[48:51], v[128:131], v[12:15]\n\t" \
    "ds_read_b64_tr_b16 v[68:69], v5 offset:32\n\t" \
    "ds_read_b64_tr_b16 v[70:71], v5 offset:2592\n\t" \
    "v_mfma_f32_16x16x32_bf16 v[32:35], v[48:51], v[132:135], v[32:35]\n\t" \
    "s_waitcnt lgkmcnt(8)\n\t" \
    "v_mfma_f32_16x16x32_bf16 v[16:19], v[52:55], v[128:131], v[16:19]\n\t" \
    "ds_read_b64_tr_b16 v[48:49], v5 offset:64\n\t" \
    "ds_read_b64_tr_b16 v[50:51], v5 offset:2624\n\t" \
    "v_mfma_f32_16x16x32_bf16 v[36:39], v[52:55], v[132:135], v[36:39]\n\t" \
    "s_waitcnt lgkmcnt(8)\n\t" \
    "v_mfma_f32_16x16x32_bf16 v[20:23], v[56:59], v[128:131], v[20:23]\n\t" \
    "ds_read_b64_tr_b16 v[52:53], v5 offset:96\n\t" \
    "ds_read_b64_tr_b16 v[54:55], v5 offset:2656\n\t" \
    "v_mfma_f32_16x16x32_bf16 v[40:43], v[56:59], v[132:135], v[40:43]\n\t" \
    "s_waitcnt lgkmcnt(8)\n\t" \
    "v_mfma_f32_16x16x32_bf16 v[24:27], v[60:63], v[128:131], v[24:27]\n\t" \
    "ds_read_b64_tr_b16 v[56:57], v5 offset:128\n\t" \
    "ds_read_b64_tr_b16 v[58:59], v5 offset:2688\n\t" \
    "v_mfma_f32_16x16x32_bf16 v[44:47], v[60:63], v[132:135], v[44:47]\n\t" \
    "s_waitcnt vmcnt(4)\n\t" \
    "OC8_%=:\n\t" \
    "v_max3_u32 v6, v136, v137, v138\n\t" \
    "v_max3_u32 v6, v6, v139, v140\n\t" \
    "v_max3_u32 v6, v6, v141, v142\n\t" \
    "v_max_u32 v6, v6, v143\n\t" \
    "v_cmp_eq_u32 vcc, -1, v6\n\t" \
    "s_and_b64 vcc, exec, vcc\n\t" \
    "s_cbranch_vccnz OF8_%=\n\t" \
    "OK8_%=:\n\t" \
    "s_waitcnt lgkmcnt(8)\n\t" \
    "v_mfma_f32_16x16x32_bf16 v[8:11], v[64:67], v[136:139], v[8:11]\n\t" \
    "ds_read_b64_tr_b16 v[60:61], v5 offset:5120\n\t" \
    "ds_read_b64_tr_b16 v[62:63], v5 offset:7680\n\t" \
    "v_mfma_f32_16x16x32_bf16 v[28:31], v[64:67], v[140:143], v[28:31]\n\t" \
    "s_waitcnt lgkmcnt(8)\n\t" \
    "v_mfma_f32_16x16x32_bf16 v[12:15], v[68:71], v[136:139], v[12:15]\n\t" \
    "ds_read_b64_tr_b16 v[64:65], v5 offset:5152\n\t" \
    "ds_read_b64_tr_b16 v[66:67], v5 offset:7712\n\t" \
    "v_mfma_f32_16x16x32_bf16 v[32:35], v[68:71], v[140:143], v[32:35]\n\t" \
    "s_waitcnt lgkmcnt(8)\n\t" \
    "v_mfma_f32_16x16x32_bf16 v[16:19], v[48:51], v[136:139], v[16:19]\n\t" \
    "ds_read_b64_tr_b16 v[68:69], v5 offset:5184\n\t" \
    "ds_read_b64_tr_b16 v[70:71], v5 offset:7744\n\t" \
    "v_mfma_f32_16x16x32_bf16 v[36:39], v[48:51], v[140:143], v[36:39]\n\t" \
    "s_waitcnt lgkmcnt(8)\n\t" \
    "v_mfma_f32_16x16x32_bf16 v[20:23], v[52:55], v[136:139], v[20:23]\n\t" \
    "ds_read_b64_tr_b16 v[48:49], v5 offset:5216\n\t" \
    "ds_read_b64_tr_b16 v[50:51], v5 offset:7776\n\t" \
    "v_mfma_f32_16x16x32_bf16 v[40:43], v[52:55], v[140:143], v[40:43]\n\t" \
    "s_waitcnt lgkmcnt(8)\n\t" \
    "v_mfma_f32_16x16x32_bf16 v[24:27], v[56:59], v[136:139], v[24:27]\n\t" \
    "ds_read_b64_tr_b16 v[52:53], v5 offset:5248\n\t" \
    "ds_read_b64_tr_b16 v[54:55], v5 offset:7808\n\t" \
    "v_mfma_f32_16x16x32_bf16 v[44:47], v[56:59], v[140:143], v[44:47]\n\t" \
    "s_waitcnt vmcnt(0)\n\t" \
    "OC9_%=:\n\t" \
    "v_max3_u32 v6, v144, v145, v146\n\t" \
    "v_max3_u32 v6, v6, v147, v148\n\t" \
    "v_max3_u32 v6, v6, v149, v150\n\t" \
    "v_max_u32 v6, v6, v151\n\t" \
    "v_cmp_eq_u32 vcc, -1, v6\n\t" \
    "s_and_b64 vcc, exec, vcc\n\t" \
    "s_cbranch_vccnz OF9_%=\n\t" \
    "OK9_%=:\n\t" \
    "s_waitcnt lgkmcnt(8)\n\t" \
    "v_mfma_f32_16x16x32_bf16 v[8:11], v[60:63], v[144:147], v[8:11]\n\t" \
    "v_mfma_f32_16x16x32_bf16 v[28:31], v[60:63], v[148:151], v[28:31]\n\t" \
    "s_waitcnt lgkmcnt(6)\n\t" \
    "v_mfma_f32_16x16x32_bf16 v[12:15], v[64:67], v[144:147], v[12:15]\n\t" \
    "v_mfma_f32_16x16x32_bf16 v[32:35], v[64:67], v[148:151], v[32:35]\n\t" \
    "s_waitcnt lgkmcnt(4)\n\t" \
    "v_mfma_f32_16x16x32_bf16 v[16:19], v[68:71], v[144:147], v[16:19]\n\t" \
    "v_mfma_f32_16x16x32_bf16 v[36:39], v[68:71], v[148:151], v[36:39]\n\t" \
    "s_waitcnt lgkmcnt(2)\n\t" \
    "v_mfma_f32_16x16x32_bf16 v[20:23], v[48:51], v[144:147], v[20:23]\n\t" \
    "v_mfma_f32_16x16x32_bf16 v[40:43], v[48:51], v[148:151], v[40:43]\n\t" \
    "s_waitcnt lgkmcnt(0)\n\t" \
    "v_mfma_f32_16x16x32_bf16 v[24:27], v[52:55], v[144:147], v[24:27]\n\t" \
    "v_mfma_f32_16x16x32_bf16 v[44:47], v[52:55], v[148:151], v[44:47]\n\t" \
    "s_nop 5\n\t" \
    "ds_write_b128 v158, v[28:31] offset:0\n\t" \
    "ds_write_b128 v158, v[32:35] offset:1024\n\t" \
    "ds_write_b128 v158, v[36:39] offset:2048\n\t" \
    "ds_write_b128 v158, v[40:43] offset:3072\n\t" \
    "ds_write_b128 v158, v[44:47] offset:4096\n\t" \
    "s_waitcnt lgkmcnt(0)\n\t" \
    "s_barrier\n\t" \
    "ds_read_b128 v[72:75], v159 offset:0\n\t" \
    "ds_read_b128 v[76:79], v159 offset:1024\n\t" \
    "ds_read_b128 v[80:83], v159 offset:2048\n\t" \
    "ds_read_b128 v[84:87], v159 offset:3072\n\t" \
    "ds_read_b128 v[88:91], v159 offset:4096\n\t" \
    "s_waitcnt lgkmcnt(4)\n\t" \
    "v_pk_add_f32 v[72:73], v[8:9], v[72:73]\n\t" \
    "v_pk_add_f32 v[74:75], v[10:11], v[74:75]\n\t" \
    "v_cvt_pk_bf16_f32 v48, v72, v73\n\t" \
    "v_cvt_pk_bf16_f32 v49, v74, v75\n\t" \
    "s_waitcnt lgkmcnt(3)\n\t" \
    "v_pk_add_f32 v[76:77], v[12:13], v[76:77]\n\t" \
    "v_pk_add_f32 v[78:79], v[14:15], v[78:79]\n\t" \
    "v_cvt_pk_bf16_f32 v50, v76, v77\n\t" \
    "v_cvt_pk_bf16_f32 v51, v78, v79\n\t" \
    "s_waitcnt lgkmcnt(2)\n\t" \
    "v_pk_add_f32 v[80:81], v[16:17], v[80:81]\n\t" \
    "v_pk_add_f32 v[82:83], v[18:19], v[82:83]\n\t" \
    "v_cvt_pk_bf16_f32 v52, v80, v81\n\t" \
    "v_cvt_pk_bf16_f32 v53, v82, v83\n\t" \
    "v_permlane16_swap_b32 v48, v50\n\t" \
    "v_permlane16_swap_b32 v49, v51\n\t" \
    "buffer_store_dwordx4 v[48:51], v162, s[48:51], 0 offen offset:0\n\t" \
    "s_waitcnt lgkmcnt(1)\n\t" \
    "v_pk_add_f32 v[84:85], v[20:21], v[84:85]\n\t" \
    "v_pk_add_f32 v[86:87], v[22:23], v[86:87]\n\t" \
    "v_cvt_pk_bf16_f32 v54, v84, v85\n\t" \
    "v_cvt_pk_bf16_f32 v55, v86, v87\n\t" \
    "s_waitcnt lgkmcnt(0)\n\t" \
    "v_pk_add_f32 v[88:89], v[24:25], v[88:89]\n\t" \
    "v_pk_add_f32 v[90:91], v[26:27], v[90:91]\n\t" \
    "v_cvt_pk_bf16_f32 v56, v88, v89\n\t" \
    "v_cvt_pk_bf16_f32 v57, v90, v91\n\t" \
    "v_permlane16_swap_b32 v52, v54\n\t" \
    "v_permlane16_swap_b32 v53, v55\n\t" \
    "buffer_store_dwordx4 v[52:55], v162, s[48:51], 0 offen offset:64\n\t" \
    "buffer_store_dwordx2 v[56:57], v163, s[48:51], 0 offen\n\t" \
    "s_branch OE_%=\n\t" \
    "OF0_%=:\n\t" \
    "s_cmp_eq_u32 s44, 0\n\t" \
    "s_cbranch_scc1 OK0_%=\n\t" \
    "s_sub_u32 s44, s44, 1\n\t" \
    "buffer_load_dwordx4 v[72:75], v0, s[36:39], s40 offen offset:0 sc1\n\t" \
    "buffer_load_dwordx4 v[76:79], v1, s[36:39], s40 offen offset:0 sc1\n\t" \
    "buffer_load_dwordx4 v[80:83], v0, s[36:39], s40 offen offset:1024 sc1\n\t" \
    "buffer_load_dwordx4 v[84:87], v1, s[36:39], s40 offen offset:1024 sc1\n\t" \
    "buffer_load_dwordx4 v[88:91], v0, s[36:39], s40 offen offset:2048 sc1\n\t" \
    "buffer_load_dwordx4 v[92:95], v1, s[36:39], s40 offen offset:2048 sc1\n\t" \
    "s_waitcnt vmcnt(0)\n\t" \
    "s_branch OC0_%=\n\t" \
    "OF1_%=:\n\t" \
    "s_cmp_eq_u32 s44, 0\n\t" \
    "s_cbranch_scc1 OK1_%=\n\t" \
    "s_sub_u32 s44, s44, 1\n\t" \
    "buffer_load_dwordx4 v[80:83], v0, s[36:39], s40 offen offset:1024 sc1\n\t" \
    "buffer_load_dwordx4 v[84:87], v1, s[36:39], s40 offen offset:1024 sc1\n\t" \
    "buffer_load_dwordx4 v[88:91], v0, s[36:39], s40 offen offset:2048 sc1\n\t" \
    "buffer_load_dwordx4 v[92:95], v1, s[36:39], s40 offen offset:2048 sc1\n\t" \
    "buffer_load_dwordx4 v[96:99], v0, s[36:39], s40 offen offset:3072 sc1\n\t" \
    "buffer_load_dwordx4 v[100:103], v1, s[36:39], s40 offen offset:3072 sc1\n\t" \
    "s_waitcnt vmcnt(0)\n\t" \
    "s_branch OC1_%=\n\t" \
    "OF2_%=:\n\t" \
    "s_cmp_eq_u32 s44, 0\n\t" \
    "s_cbranch_scc1 OK2_%=\n\t" \
    "s_sub_u32 s44, s44, 1\n\t" \
    "buffer_load_dwordx4 v[88:91], v0, s[36:39], s40 offen offset:2048 sc1\n\t" \
    "buffer_load_dwordx4 v[92:95], v1, s[36:39], s40 offen offset:2048 sc1\n\t" \
    "buffer_load_dwordx4 v[96:99], v0, s[36:39], s40 offen offset:3072 sc1\n\t" \
    "buffer_load_dwordx4 v[100:103], v1, s[36:39], s40 offen offset:3072 sc1\n\t" \
    "buffer_load_dwordx4 v[104:107], v0, s[36:39], s41 offen offset:0 sc1\n\t" \
    "buffer_load_dwordx4 v[108:111], v1, s[36:39], s41 offen offset:0 sc1\n\t" \
    "s_waitcnt vmcnt(0)\n\t" \
    "s_branch OC2_%=\n\t" \
    "OF3_%=:\n\t" \
    "s_cmp_eq_u32 s44, 0\n\t" \
    "s_cbranch_scc1 OK3_%=\n\t" \
    "s_sub_u32 s44, s44, 1\n\t" \
    "buffer_load_dwordx4 v[96:99], v0, s[36:39], s40 offen offset:3072 sc1\n\t" \
    "buffer_load_dwordx4 v[100:103], v1, s[36:39], s40 offen offset:3072 sc1\n\t" \
    "buffer_load_dwordx4 v[104:107], v0, s[36:39], s41 offen offset:0 sc1\n\t" \
    "buffer_load_dwordx4 v[108:111], v1, s[36:39], s41 offen offset:0 sc1\n\t" \
    "buffer_load_dwordx4 v[112:115], v0, s[36:39], s41 offen offset:1024 sc1\n\t" \
    "buffer_load_dwordx4 v[116:119], v1, s[36:39], s41 offen offset:1024 sc1\n\t" \
    "s_waitcnt vmcnt(0)\n\t" \
    "s_branch OC3_%=\n\t" \
    "OF4_%=:\n\t" \
    "s_cmp_eq_u32 s44, 0\n\t" \
    "s_cbranch_scc1 OK4_%=\n\t" \
    "s_sub_u32 s44, s44, 1\n\t" \
    "buffer_load_dwordx4 v[104:107], v0, s[36:39], s41 offen offset:0 sc1\n\t" \
    "buffer_load_dwordx4 v[108:111], v1, s[36:39], s41 offen offset:0 sc1\n\t" \
    "buffer_load_dwordx4 v[112:115], v0, s[36:39], s41 offen offset:1024 sc1\n\t" \
    "buffer_load_dwordx4 v[116:119], v1, s[36:39], s41 offen offset:1024 sc1\n\t" \
    "buffer_load_dwordx4 v[120:123], v0, s[36:39], s41 offen offset:2048 sc1\n\t" \
    "buffer_load_dwordx4 v[124:127], v1, s[36:39], s41 offen offset:2048 sc1\n\t" \
    "s_waitcnt vmcnt(0)\n\t" \
    "s_branch OC4_%=\n\t" \
    "OF5_%=:\n\t" \
    "s_cmp_eq_u32 s44, 0\n\t" \
    "s_cbranch_scc1 OK5_%=\n\t" \
    "s_sub_u32 s44, s44, 1\n\t" \
    "buffer_load_dwordx4 v[112:115], v0, s[36:39], s41 offen offset:1024 sc1\n\t" \
    "buffer_load_dwordx4 v[116:119], v1, s[36:39], s41 offen offset:1024 sc1\n\t" \
    "buffer_load_dwordx4 v[120:123], v0, s[36:39], s41 offen offset:2048 sc1\n\t" \
    "buffer_load_dwordx4 v[124:127], v1, s[36:39], s41 offen offset:2048 sc1\n\t" \
    "buffer_load_dwordx4 v[128:131], v0, s[36:39], s41 offen offset:3072 sc1\n\t" \
    "buffer_load_dwordx4 v[132:135], v1, s[36:39], s41 offen offset:3072 sc1\n\t" \
    "s_waitcnt vmcnt(0)\n\t" \
    "s_branch OC5_%=\n\t" \
    "OF6_%=:\n\t" \
    "s_cmp_eq_u32 s44, 0\n\t" \
    "s_cbranch_scc1 OK6_%=\n\t" \
    "s_sub_u32 s44, s44, 1\n\t" \
    "buffer_load_dwordx4 v[120:123], v0, s[36:39], s41 offen offset:2048 sc1\n\t" \
    "buffer_load_dwordx4 v[124:127], v1, s[36:39], s41 offen offset:2048 sc1\n\t" \
    "buffer_load_dwordx4 v[128:131], v0, s[36:39], s41 offen offset:3072 sc1\n\t" \
    "buffer_load_dwordx4 v[132:135], v1, s[36:39], s41 offen offset:3072 sc1\n\t" \
    "buffer_load_dwordx2 v[136:137], v2, s[36:39], s40 offen offset:0 sc1\n\t" \
    "buffer_load_dwordx2 v[138:139], v2, s[36:39], s40 offen offset:512 sc1\n\t" \
    "buffer_load_dwordx2 v[140:141], v3, s[36:39], s40 offen offset:0 sc1\n\t" \
    "buffer_load_dwordx2 v[142:143], v3, s[36:39], s40 offen offset:512 sc1\n\t" \
    "s_waitcnt vmcnt(0)\n\t" \
    "s_branch OC6_%=\n\t" \
    "OF7_%=:\n\t" \
    "s_cmp_eq_u32 s44, 0\n\t" \
    "s_cbranch_scc1 OK7_%=\n\t" \
    "s_sub_u32 s44, s44, 1\n\t" \
    "buffer_load_dwordx4 v[128:131], v0, s[36:39], s41 offen offset:3072 sc1\n\t" \
    "buffer_load_dwordx4 v[132:135], v1, s[36:39], s41 offen offset:3072 sc1\n\t" \
    "buffer_load_dwordx2 v[136:137], v2, s[36:39], s40 offen offset:0 sc1\n\t" \
    "buffer_load_dwordx2 v[138:139], v2, s[36:39], s40 offen offset:512 sc1\n\t" \
    "buffer_load_dwordx2 v[140:141], v3, s[36:39], s40 offen offset:0 sc1\n\t" \
    "buffer_load_dwordx2 v[142:143], v3, s[36:39], s40 offen offset:512 sc1\n\t" \
    "buffer_load_dwordx2 v[144:145], v2, s[36:39], s41 offen offset:0 sc1\n\t" \
    "buffer_load_dwordx2 v[146:147], v2, s[36:39], s41 offen offset:512 sc1\n\t" \
    "buffer_load_dwordx2 v[148:149], v3, s[36:39], s41 offen offset:0 sc1\n\t" \
    "buffer_load_dwordx2 v[150:151], v3, s[36:39], s41 offen offset:512 sc1\n\t" \
    "s_waitcnt vmcnt(0)\n\t" \
    "s_branch OC7_%=\n\t" \
    "OF8_%=:\n\t" \
    "s_cmp_eq_u32 s44, 0\n\t" \
    "s_cbranch_scc1 OK8_%=\n\t" \
    "s_sub_u32 s44, s44, 1\n\t" \
    "buffer_load_dwordx2 v[136:137], v2, s[36:39], s40 offen offset:0 sc1\n\t" \
    "buffer_load_dwordx2 v[138:139], v2, s[36:39], s40 offen offset:512 sc1\n\t" \
    "buffer_load_dwordx2 v[140:141], v3, s[36:39], s40 offen offset:0 sc1\n\t" \
    "buffer_load_dwordx2 v[142:143], v3, s[36:39], s40 offen offset:512 sc1\n\t" \
    "buffer_load_dwordx2 v[144:145], v2, s[36:39], s41 offen offset:0 sc1\n\t" \
    "buffer_load_dwordx2 v[146:147], v2, s[36:39], s41 offen offset:512 sc1\n\t" \
    "buffer_load_dwordx2 v[148:149], v3, s[36:39], s41 offen offset:0 sc1\n\t" \
    "buffer_load_dwordx2 v[150:151], v3, s[36:39], s41 offen offset:512 sc1\n\t" \
    "s_waitcnt vmcnt(0)\n\t" \
    "s_branch OC8_%=\n\t" \
    "OF9_%=:\n\t" \
    "s_cmp_eq_u32 s44, 0\n\t" \
    "s_cbranch_scc1 OK9_%=\n\t" \
    "s_sub_u32 s44, s44, 1\n\t" \
    "buffer_load_dwordx2 v[144:145], v2, s[36:39], s41 offen offset:0 sc1\n\t" \
    "buffer_load_dwordx2 v[146:147], v2, s[36:39], s41 offen offset:512 sc1\n\t" \
    "buffer_load_dwordx2 v[148:149], v3, s[36:39], s41 offen offset:0 sc1\n\t" \
    "buffer_load_dwordx2 v[150:151], v3, s[36:39], s41 offen offset:512 sc1\n\t" \
    "s_waitcnt vmcnt(0)\n\t" \
    "s_branch OC9_%=\n\t" \
    "OE_%=:\n\t" \
    : "+{v157}"(probe) \
    : "{v[0:1]}"(vO), "{v[2:3]}"(vO2), "{v[4:5]}"(wb), "{v156}"(vP), "{v7}"(vS), "{v[158:159]}"(xa), "{v[162:163]}"(da), "{s[36:39]}"(osrd), "{s[40:41]}"(ko), "{s45}"(ld), "{s[48:51]}"(outsrd) \
    : "v6", "v8", "v9", "v10", "v11", "v12", "v13", "v14", "v15", "v16", "v17", "v18", "v19", "v20", "v21", "v22", "v23", "v24", "v25", "v26", "v27", "v28", "v29", "v30", "v31", "v32", "v33", "v34", "v35", "v36", "v37", "v38", "v39", "v40", "v41", "v42", "v43", "v44", "v45", "v46", "v47", "v48", "v49", "v50", "v51", "v52", "v53", "v54", "v55", "v56", "v57", "v58", "v59", "v60", "v61", "v62", "v63", "v64", "v65", "v66", "v67", "v68", "v69", "v70", "v71", "v72", "v73", "v74", "v75", "v76", "v77", "v78", "v79", "v80", "v81", "v82", "v83", "v84", "v85", "v86", "v87", "v88", "v89", "v90", "v91", "v92", "v93", "v94", "v95", "v96", "v97", "v98", "v99", "v100", "v101", "v102", "v103", "v104", "v105", "v106", "v107", "v108", "v109", "v110", "v111", "v112", "v113", "v114", "v115", "v116", "v117", "v118", "v119", "v120", "v121", "v122", "v123", "v124", "v125", "v126", "v127", "v128", "v129", "v130", "v131", "v132", "v133", "v134", "v135", "v136", "v137", "v138", "v139", "v140", "v141", "v142", "v143", "v144", "v145", "v146", "v147", "v148", "v149", "v150", "v151", "s44", "s46", "vcc", "scc", "memory")


__device__ __forceinline__ u32x4 make_srd(const void* p, unsigned bytes) {
  u64 a = (u64)p;
  u32x4 r = {(unsigned)a, (unsigned)(a >> 32) & 0xffffu, bytes, 0x00020000u};
  return r;
}
__device__ __forceinline__ float half_max(float v) {
  auto s = __builtin_amdgcn_permlane32_swap(__builtin_bit_cast(unsigned, v), __builtin_bit_cast(unsigned, v), false, false);
  return fmaxf(__builtin_bit_cast(float, (unsigned)s[0]), __builtin_bit_cast(float, (unsigned)s[1]));
}

constexpr unsigned KV_HEAD_B = 163840, KV_B = 64 * KV_HEAD_B, O_B = 256 * 20480;
constexpr unsigned FS = 128;
constexpr unsigned EPOCH = 2048 * FS, HINT = EPOCH + 512;
constexpr unsigned FLAG_B = HINT + 256 * FS;
constexpr unsigned RING = 81920;

struct Args {
  const char* x[2];
  const char* w[2];
  const char* wo[2];
  char* ws;
  char* out;
};

__device__ __forceinline__ f32x16 attend_exact(__amdgpu_buffer_rsrc_t kv, unsigned head_off, u32x4 qm, u32x2 qr, int lane, unsigned vones) {
  const int r = lane & 31, hh = lane >> 5;
  f32x16 o;
  for (int i = 0; i < 16; ++i) o[i] = 0.f;
  float m = -__builtin_inff();
  const f32x16 zero = o;
  if (hh) qr = (u32x2){0u, 0u};
#pragma unroll 1
  for (int t = 0; t < 64; ++t) {
    const unsigned base = head_off + (t >> 1) * 5120, p = t & 1;
    u32x4 km = __builtin_amdgcn_raw_buffer_load_b128(kv, base + p * 1024 + lane * 16, 0, AUX_SC1);
    u32x2 kr = __builtin_amdgcn_raw_buffer_load_b64(kv, base + 2048 + r * 16 + p * 8, 0, AUX_SC1);
    const unsigned vo = base + 2560 + p * 1280 + hh * 320 + (r < 20 ? r : 0) * 16;
    u32x4 v0 = __builtin_amdgcn_raw_buffer_load_b128(kv, vo, 0, AUX_SC1);
    u32x4 v1 = __builtin_amdgcn_raw_buffer_load_b128(kv, vo + 640, 0, AUX_SC1);
    if (hh) kr = (u32x2){0u, 0u};
    if (r >= 20) { v0 = (u32x4){vones, vones, vones, vones}; v1 = v0; }
    f32x16 s = __builtin_amdgcn_mfma_f32_32x32x16_bf16(__builtin_bit_cast(bf16x8, km), __builtin_bit_cast(bf16x8, qm), zero, 0, 0, 0);
    s = __builtin_amdgcn_mfma_f32_32x32x8bf16_1k(__builtin_bit_cast(s16x4, kr), __builtin_bit_cast(s16x4, qr), s, 0, 0, 0);
    float tm = s[0];
    for (int i = 1; i < 16; ++i) tm = fmaxf(tm, s[i]);
    const float mn = fmaxf(m, half_max(tm));
    const float alpha = __builtin_amdgcn_exp2f(m - mn);
    m = mn;
    f32x8 p0, p1;
    for (int i = 0; i < 8; ++i) {
      p0[i] = __builtin_amdgcn_exp2f(s[i] - mn);
      p1[i] = __builtin_amdgcn_exp2f(s[8 + i] - mn);
    }
    for (int i = 0; i < 16; ++i) o[i] *= alpha;
    o = __builtin_amdgcn_smfmac_f32_32x32x32_bf16(__builtin_bit_cast(bf16x8, v0), __builtin_bit_cast(bf16x16, p0), o, 0xdddd, 0, 0);
    o = __builtin_amdgcn_smfmac_f32_32x32x32_bf16(__builtin_bit_cast(bf16x8, v1), __builtin_bit_cast(bf16x16, p1), o, 0xdddd, 0, 0);
  }
  return o;
}

#define TAKE_ARGS \
  asm volatile("" :: "s"(args.wo[0]), "s"(args.wo[1]), "s"(args.ws), "s"(args.out)); \
  const char* const woin = b ? args.wo[1] : args.wo[0]; \
  char* const ws = args.ws; \
  char* const outp = args.out; \
  char* const flags = ws + KV_B + O_B; \
  u64* const epoch = (u64*)(flags + EPOCH + b * 256); \
  char* const kvp = ws + (size_t)(b * 32 + hp * 2) * KV_HEAD_B;

__global__ void __launch_bounds__(768, 3) mha2_kernel(Args args) {
  __shared__ __attribute__((aligned(16))) char lds[163840];
  const int bid = blockIdx.x;
  u64 ep;
  LOADER_G(threadIdx.x, __builtin_amdgcn_kernarg_segment_ptr(), bid, ep);
  const int xcd = bid & 7, wi = bid >> 3;
  const int b = xcd >> 2, hp = (xcd & 1) * 8 + (wi & 7), tb = ((xcd >> 1) & 1) * 4 + (wi >> 3);
  const int otb = (tb & 4) | ((xcd & 1) << 1) | (tb & 1), th = (tb >> 1) & 1, cb = hp & 7;
  const int lane = threadIdx.x & 63;
  const int wave = __builtin_amdgcn_readfirstlane(threadIdx.x >> 6) - 4;
  const unsigned ldsb = (unsigned)(uintptr_t)lds;
  if (wave < 0) {
    const int li = wave + 4;
    TAKE_ARGS
    const unsigned sli = li;
    int ln = lane;
    asm volatile("" : "+v"(ln));
    __builtin_assume(ln >= 0 && ln < 64);
    u32x16 misc;
    for (int i = 0; i < 5; ++i) {
      const int n = i * 64 + ln, lr = n / 10, t = lr >> 4, gg = (lr >> 2) & 3, qq = lr & 3;
      const int col = cb * 160 + (n - lr * 10) * 16 - (i & 3) * 1024;
      misc[i] = (20 * (gg >> 1) + 4 * (gg & 1) + 8 * t + qq) * 1280 + col;
      misc[5 + i] = (40 * gg + 20 * t + 16 + qq) * 1280 + col;
    }
    const unsigned km = ldsb + RING + (li & 1) * 20480 + (li >> 1) * 10240;
    misc[10] = li * 4 * 20480 + ((hp >> 2) * 5 + (hp & 3)) * 1024 + ln * 16;
    misc[11] = (li & 1) * KV_HEAD_B + (li >> 1) * 10240 + ln * 16;
    misc[12] = km + ln * 16;
    misc[13] = ((ln & 7) ^ tb) * FS;
    misc[14] = (li * 4 + (ln >> 4)) * 20480 + (hp >> 2) * 5120 + 4096 + ((ln >> 3) & 1) * 512 + (hp & 3) * 128 + (ln & 7) * 16;
    misc[15] = (((ln & 31) >> 1) * 4 + 2 * th + (ln & 1)) * FS;
    const u64 tag = ep * 6364136223846793005ull + 1442695040888963407ull;
    const u32x2 vtag = {(unsigned)tag, (unsigned)(tag >> 32)};
    const u32x4 wosrd = make_srd(woin, 640u * 1280u);
    const u32x4 kvsrd = make_srd(kvp, 2 * KV_HEAD_B), fsrd = make_srd(flags, FLAG_B);
    u32x2 sw = {ldsb + li * 5120, (unsigned)li * (40 * 1280)};
    const unsigned poff = ((b * 16 + hp) * 4 + li) * (8 * FS), foff = poff + tb * FS, wofr = li * (160 * 1280), stb = tb;
    const u32x4 osrd = make_srd(ws + KV_B + (size_t)(b * 128 + tb * 16) * 20480, 16 * 20480);
    const u32x2 spf = {(1024 + (b * 8 + tb) * 64 + hp * 4 + li) * FS, (1024 + (b * 8 + otb) * 64) * FS};
    const unsigned hoff = HINT + ((b * 8 + tb) * 16 + hp) * FS;
    LOADER_A(vtag, misc, kvsrd, fsrd, wosrd, sw, km, stb, foff, poff, wofr, sli, osrd, spf, ldsb, hoff);
    return;
  }

  TAKE_ARGS
  const int g = wave & 1, tg = wave >> 1;
  const int r = lane & 31, hh = lane >> 5;
  f32x16 o[2];
  u32x4 qm[2], km0;
  u32x2 qr[2], kr0;
  const u64 vmask = 0x000fffff000fffffull, lomask = 0xffffffffull;
  u32x4 selm;
  u32x2 selr;
  for (int i = 0; i < 4; ++i) selm[i] = r == 8 * hh + 2 * i ? 0x3f80u : r == 8 * hh + 2 * i + 1 ? 0x3f800000u : 0u;
  for (int i = 0; i < 2; ++i) selr[i] = lane == 48 + 2 * i ? 0x3f80u : lane == 49 + 2 * i ? 0x3f800000u : 0u;
  const unsigned vones = (r == 20 || r == 24) ? 0x3f803f80u : 0u;
  const u32x2 ost = {(unsigned)(tg * 4 + (r >> 4)) * 20480 + ((hp >> 2) * 5 + (hp & 3)) * 1024 + (2 * g + hh) * 256 + (r & 15) * 16,
               (unsigned)(tg * 4 + (r >> 4)) * 20480 + (hp >> 2) * 5120 + 4096 + g * 512 + (hp & 3) * 128 + (r & 15) * 8};
  const int tp = wave & 3, kh = wave >> 2;
  const unsigned oblk = (b * 128 + otb * 16) * 20480;
  const unsigned vP = O_B - oblk + HINT + ((b * 8 + otb) * 16 + kh * 8 + (lane & 7)) * FS;
  const u32x4 osrd = make_srd(ws + KV_B + oblk, O_B - oblk + FLAG_B);
  const u32x4 owsrd = make_srd(ws + KV_B + (size_t)(b * 128 + tb * 16) * 20480, 16 * 20480);
  const unsigned hw = wave == 0 ? O_B - oblk + HINT + ((b * 8 + tb) * 16 + hp) * FS : 0;
  const unsigned aK0 = ldsb + RING + g * 20480 + lane * 16, aV0 = ldsb + RING + g * 20480 + 2560 + hh * 320 + r * 16;
  {
    u32x2 xa;
    u32x4 wa;
    const int row = tg * 64 + (lane & 15), kg = lane >> 4, q = (lane >> 2) & 3, p = lane & 3;
    for (int k = 0; k < 2; ++k) xa[k] = ldsb + row * 128 + (((4 * k + kg) ^ ((row >> 1) & 7)) << 4);
    for (int f = 0; f < 4; ++f) {
      const int seg = f < 3 ? f : p < 2 ? p : 2, u = 5 * g + (f < 3 ? p : 4);
      const u64 tab = seg == 0 ? 0x3002000301ull : seg == 1 ? 0x3113111210ull : 0x3332202322ull;
      const int e = (int)((tab >> (8 * (u >> 1))) & 0xff);
      wa[f] = ldsb + 32768 + kg * 2048 + ((e >> 4) * 16 + 4 * q + ((e & 15) ^ (3 * (kg & 1)))) * 16 + (u & 1) * 8;
    }
    const int rs = (r & 19) | ((r & 4) << 1) | ((r & 8) >> 1);
    const unsigned img = ldsb + RING + g * 20480 + tg * 5120;
    const unsigned akw = img + (hh * 32 + rs) * 16, avw = img + 2560 + hh * 320 + r * 16;
    const float sc = 0.32259642f, cr = lane < 16 ? sc : 1.0f;
    qm[0] = selm;
    qm[1] = (u32x4){selr[0], selr[1], 0u, 0u};
    GEMM_CORE(o, xa, wa, qm, qr, km0, kr0, cr, akw, avw, sc, vmask, lomask, ost, aK0, aV0, vones, vP, osrd, hw, owsrd);
  }
  const __amdgpu_buffer_rsrc_t kvr = __builtin_amdgcn_make_buffer_rsrc(kvp, 0, 2 * KV_HEAD_B, 0x00020000);

  u64 bad;
  unsigned probe;
  ATTN_CORE(o, qm, qr, km0, kr0, bad, probe, vP, osrd, aK0, aV0, vones, vmask, lomask, hw, ost, owsrd);
  if (__builtin_expect(bad != 0, 0)) {
    o[0] = attend_exact(kvr, g * KV_HEAD_B, qm[0], qr[0], lane, vones);
    o[1] = attend_exact(kvr, g * KV_HEAD_B, qm[1], qr[1], lane, vones);
    asm volatile("s_nop 15" : "+{v[104:119]}"(o[0]), "+{v[120:135]}"(o[1]));
  }
  PUBLISH_O(o, probe, ost, owsrd, osrd, hw, lomask);

  {
    int ln = lane;
    asm volatile("" : "+v"(ln));
    __builtin_assume(ln >= 0 && ln < 64);
    const int og = ln >> 4;
    const unsigned t0 = (th * 8 + tp * 2 + kh) * 20480, t1 = (th * 8 + tp * 2 + 1 - kh) * 20480;
    const u32x2 vO = {t0 + ln * 16, t1 + ln * 16}, vO2 = {t0 + 4096 + ln * 8, t1 + 4096 + ln * 8};
    const unsigned wl = ldsb + (ln >> 2) * 160 + (ln & 3) * 8;
    const u32x2 wb = {wl + kh * 40960, wl + RING + kh * 10240};
    const u32x2 ko = {kh * 10240u, kh * 10240u + 5120u};
    const unsigned vS = ldsb + RING + 20480 + kh * 4;
    const u32x2 xa = {ldsb + RING + 40960 + wave * 5120 + ln * 16, ldsb + RING + 40960 + (wave ^ 4) * 5120 + ln * 16};
    const unsigned dst = (otb * 256 + th * 128 + tp * 32 + kh * 16 + (ln & 15)) * 2560 + b * 1280 + cb * 160;
    const u32x2 da = {dst + (og & 1) * 32 + (og >> 1) * 16, dst + 128 + og * 8};
    const u32x4 outsrd = make_srd(outp, 2048 * 2560);
    OPROJ_CORE(vO, vO2, wb, vP, vS, xa, da, probe, osrd, ko, tp, outsrd);
  }
  if (hp == 0 && tb == 0 && threadIdx.x == 256)
    __hip_atomic_store(epoch, *(const u64 LDSP*)((char LDSP*)lds + RING + 20480 + 8), __ATOMIC_RELAXED, __HIP_MEMORY_SCOPE_AGENT);
}

extern "C" void kernel_launch(void* const* d_in, const int* in_sizes, int n_in, void* d_out, int out_size, void* d_ws,
                              size_t ws_size, hipStream_t stream) {
  Args a;
  a.x[0] = (const char*)d_in[0];
  a.w[0] = (const char*)d_in[1];
  a.wo[0] = (const char*)d_in[2];
  a.x[1] = (const char*)d_in[3];
  a.w[1] = (const char*)d_in[4];
  a.wo[1] = (const char*)d_in[5];
  a.ws = (char*)d_ws;
  a.out = (char*)d_out;
  mha2_kernel<<<256, 768, 0, stream>>>(a);
}
```
